```python
import torch

def _softmax(x, axis=-1):
    return torch.softmax(x, dim=axis)
def _gelu(x):
    return torch.nn.functional.gelu(x, approximate="tanh")
def _silu(x):
    return torch.nn.functional.silu(x)
def _sigmoid(x):
    return torch.sigmoid(x)
def _layernorm(x, axis=-1, eps=1e-5):
    m = x.mean(dim=axis, keepdim=True)
    v = x.var(dim=axis, keepdim=True, unbiased=False)
    return (x - m) / torch.sqrt(v + eps)
def _scatter_add(tgt, idx, src, axis=0):
    return tgt.clone().index_add_(axis, idx, src)
def _segment_sum(src, seg, n):
    out = torch.zeros((n,) + src.shape[1:], dtype=src.dtype)
    return out.index_add_(0, seg, src)
def _pad(x, pw):
    flat = tuple(v for pair in reversed(pw) for v in pair)
    return torch.nn.functional.pad(x, flat)

kernel_name = 'standalone__gqa_block_cached__near_ridge'

def setup_inputs():

    _w = (lambda a: (lambda t: t.to(torch.bfloat16) if t.is_floating_point() else t)(torch.as_tensor(a)))
    B, Sq, D = 16, 64, 512
    Hq, Hkv, Sk, Dh = 32, 8, 1024, 128
    assert Hq % Hkv == 0, (Hq, Hkv)
    return {
        "x": _w((lambda *s: torch.randn(*s, dtype=torch.bfloat16) * 0.8)(B, Sq, D)),
        "k_cache": _w((lambda *s: torch.randn(*s, dtype=torch.bfloat16) * 0.8)(B, Hkv, Sk, Dh)),
        "v_cache": _w((lambda *s: torch.randn(*s, dtype=torch.bfloat16) * 0.05)(B, Hkv, Sk, Dh)),
        "wq": _w((lambda *s: torch.randn(*s, dtype=torch.bfloat16) * 0.8)(D, Hq * Dh) * (D ** -0.5)),
        "wo": _w((lambda *s: torch.randn(*s, dtype=torch.bfloat16) * 0.05)(Hq * Dh, D) * ((Hq * Dh) ** -0.5)),
    }

def reference(x, k_cache, v_cache, wq, wo):
    B, Sq, D = x.shape
    Hq, Dh = 32, 128
    Hkv = k_cache.shape[1]
    rep = Hq // Hkv
    q = (lambda x, *d: x.permute(*d))((x @ wq).reshape(B, Sq, Hq, Dh), 0, 2, 1, 3)
    k = (lambda x, n, axis: torch.repeat_interleave(x, n, dim=axis))(k_cache, rep, axis=1)
    v = (lambda x, n, axis: torch.repeat_interleave(x, n, dim=axis))(v_cache, rep, axis=1)
    scores = torch.einsum("bhqd,bhkd->bhqk", q, k) * (Dh ** -0.5)
    p = _softmax(scores, axis=-1)
    o = torch.einsum("bhqk,bhkd->bhqd", p, v)
    return (lambda x, *d: x.permute(*d))(o, 0, 2, 1, 3).reshape(B, Sq, Hq * Dh) @ wo
```

```cpp
#include <hip/hip_runtime.h>
#include <hip/hip_bf16.h>
#include <stdint.h>

typedef unsigned short u16;
__device__ __forceinline__ float bf2f(u16 x) { return __uint_as_float(((unsigned)x) << 16); }
__device__ __forceinline__ u16 f2bf(float f) { unsigned u = __float_as_uint(f); u = (u + 0x7FFF + ((u >> 16) & 1)) >> 16; return (u16)u; }

constexpr int B = 16, SQ = 64, D = 512, HQ = 32, HKV = 8, SK = 1024, DH = 128;

__global__ void gemm_naive(const u16* __restrict__ A, const u16* __restrict__ Bm, u16* __restrict__ C, int M, int N, int K) {
  __shared__ float As[64][17], Bs[16][65];
  int tx = threadIdx.x & 15, ty = threadIdx.x >> 4;
  int m0 = blockIdx.y * 64, n0 = blockIdx.x * 64;
  float acc[4][4] = {};
  for (int k0 = 0; k0 < K; k0 += 16) {
    for (int i = threadIdx.x; i < 64 * 16; i += 256) { int r = i >> 4, c = i & 15; As[r][c] = bf2f(A[(size_t)(m0 + r) * K + k0 + c]); }
    for (int i = threadIdx.x; i < 16 * 64; i += 256) { int r = i >> 6, c = i & 63; Bs[r][c] = bf2f(Bm[(size_t)(k0 + r) * N + n0 + c]); }
    __syncthreads();
    for (int k = 0; k < 16; ++k) {
      float a[4], b[4];
      for (int i = 0; i < 4; ++i) a[i] = As[ty * 4 + i][k];
      for (int j = 0; j < 4; ++j) b[j] = Bs[k][tx * 4 + j];
      for (int i = 0; i < 4; ++i) for (int j = 0; j < 4; ++j) acc[i][j] += a[i] * b[j];
    }
    __syncthreads();
  }
  for (int i = 0; i < 4; ++i) for (int j = 0; j < 4; ++j) C[(size_t)(m0 + ty * 4 + i) * N + n0 + tx * 4 + j] = f2bf(acc[i][j]);
}

__global__ void attn_naive(const u16* __restrict__ Q, const u16* __restrict__ Kc, const u16* __restrict__ Vc, u16* __restrict__ O) {
  int q = blockIdx.x & 63, h = (blockIdx.x >> 6) & 31, b = blockIdx.x >> 11;
  int kvh = h >> 2;
  __shared__ float qs[128], p[1024], red[128];
  int t = threadIdx.x;
  qs[t] = bf2f(Q[((size_t)(b * SQ + q)) * (HQ * DH) + h * DH + t]);
  __syncthreads();
  const u16* Kb = Kc + (size_t)(b * HKV + kvh) * SK * DH;
  const u16* Vb = Vc + (size_t)(b * HKV + kvh) * SK * DH;
  float mx = -1e30f;
  for (int k = t; k < SK; k += 128) {
    float s = 0; for (int d = 0; d < DH; ++d) s += qs[d] * bf2f(Kb[(size_t)k * DH + d]);
    s *= 0.08838834764831845f; p[k] = s; mx = fmaxf(mx, s);
  }
  red[t] = mx; __syncthreads();
  for (int o = 64; o > 0; o >>= 1) { if (t < o) red[t] = fmaxf(red[t], red[t + o]); __syncthreads(); }
  mx = red[0]; __syncthreads();
  float sm = 0;
  for (int k = t; k < SK; k += 128) { float e = __expf(p[k] - mx); p[k] = e; sm += e; }
  red[t] = sm; __syncthreads();
  for (int o = 64; o > 0; o >>= 1) { if (t < o) red[t] += red[t + o]; __syncthreads(); }
  sm = red[0];
  float o = 0; for (int k = 0; k < SK; ++k) o += p[k] * bf2f(Vb[(size_t)k * DH + t]);
  O[((size_t)(b * SQ + q)) * (HQ * DH) + h * DH + t] = f2bf(o / sm);
}

extern "C" void kernel_launch(void* const* d_in, const int* in_sizes, int n_in, void* d_out, int out_size, void* d_ws, size_t ws_size, hipStream_t stream) {
  const u16* x = (const u16*)d_in[0]; const u16* kc = (const u16*)d_in[1]; const u16* vc = (const u16*)d_in[2];
  const u16* wq = (const u16*)d_in[3]; const u16* wo = (const u16*)d_in[4];
  u16* Q = (u16*)d_ws; u16* O = Q + (size_t)B * SQ * HQ * DH;
  gemm_naive<<<dim3(4096 / 64, 1024 / 64), 256, 0, stream>>>(x, wq, Q, 1024, 4096, 512);
  attn_naive<<<B * HQ * SQ, 128, 0, stream>>>(Q, kc, vc, O);
  gemm_naive<<<dim3(512 / 64, 1024 / 64), 256, 0, stream>>>(O, wo, (u16*)d_out, 1024, 512, 4096);
}
```

```cpp
#include <hip/hip_runtime.h>
#include <stdint.h>

typedef __bf16 bf16x8 __attribute__((ext_vector_type(8)));
typedef __bf16 bf16x4 __attribute__((ext_vector_type(4)));
typedef __bf16 bf16x2 __attribute__((ext_vector_type(2)));
typedef float f32x16 __attribute__((ext_vector_type(16)));
typedef float f32x4 __attribute__((ext_vector_type(4)));
typedef float f32x2 __attribute__((ext_vector_type(2)));
typedef unsigned u32x4 __attribute__((ext_vector_type(4)));
typedef unsigned u32x2 __attribute__((ext_vector_type(2)));
#define LAS __attribute__((address_space(3)))
#define GAS __attribute__((address_space(1)))
#define DI __device__ __forceinline__

constexpr int NB = 16, SQ = 64, DM = 512, HQ = 32, HKV = 8, SK = 1024, DH = 128;
constexpr unsigned SLOT = 16384u;
constexpr unsigned LDS_BYTES = 10 * SLOT;
constexpr float QSCALE = 0.12751743082f;
constexpr float THR = 8.0f;

#define BAR() asm volatile("s_waitcnt lgkmcnt(0)\n\ts_barrier" ::: "memory")
#define HBAR() asm volatile("s_barrier" ::: "memory")
#define VMW(n) asm volatile("s_waitcnt vmcnt(%0)" :: "n"(n) : "memory")
#define SBAR() __builtin_amdgcn_sched_barrier(0)

DI unsigned cvtpk(float lo, float hi) { f32x2 v = {lo, hi}; bf16x2 b = __builtin_convertvector(v, bf16x2); return __builtin_bit_cast(unsigned, b); }
DI bf16x8 ldsb128(unsigned a) { return *(const LAS bf16x8*)(uintptr_t)a; }
DI bf16x8 ldtr2(unsigned a0, unsigned a1) {
  bf16x4 lo = __builtin_amdgcn_ds_read_tr16_b64_v4bf16((LAS bf16x4*)(uintptr_t)a0);
  bf16x4 hi = __builtin_amdgcn_ds_read_tr16_b64_v4bf16((LAS bf16x4*)(uintptr_t)a1);
  return __builtin_shufflevector(lo, hi, 0, 1, 2, 3, 4, 5, 6, 7);
}
DI f32x16 mfma(bf16x8 a, bf16x8 b, f32x16 c) { return __builtin_amdgcn_mfma_f32_32x32x16_bf16(a, b, c, 0, 0, 0); }

DI void dma4(const char* g, unsigned voff, unsigned gstep, unsigned ldsaddr) {
#pragma unroll
  for (int i = 0; i < 4; ++i)
    __builtin_amdgcn_global_load_lds((const GAS void*)(g + (size_t)i * gstep + voff), (LAS void*)(uintptr_t)(ldsaddr + i * 4096u), 16, 0, 0);
}

__device__ constexpr unsigned wq_slot(int i) { return (6u + (unsigned)(i & 3)) * SLOT; }
__device__ constexpr unsigned x_slot(int c) { return (c == 0 ? 4u : c == 1 ? 2u : c == 2 ? 5u : 3u) * SLOT; }
__device__ constexpr unsigned wo_slot(int n) { return ((n & 7) == 0 ? 8u : (n & 7) == 1 ? 9u : (n & 7) == 2 ? 0u : (n & 7) == 3 ? 1u : (unsigned)(n & 7)) * SLOT; }

__global__ __launch_bounds__(512, 2) void fused_kernel(const char* __restrict__ x, const char* __restrict__ kc, const char* __restrict__ vc,
                                                       const char* __restrict__ wq, const char* __restrict__ wo, char* __restrict__ slab) {
  extern __shared__ __attribute__((aligned(16))) char lds[];
  const unsigned ldsb = (unsigned)(uintptr_t)lds;
  const unsigned lane = threadIdx.x & 63u;
  const unsigned wid = __builtin_amdgcn_readfirstlane(threadIdx.x >> 6);
  const unsigned kvh = blockIdx.x & 7u, half = (blockIdx.x >> 3) & 1u, b = blockIdx.x >> 4;

  if (wid >= 4) {
    const unsigned hw = wid - 4;
    const char* xb = x + (size_t)b * SQ * DM * 2;
    const char* kb = kc + (size_t)(b * HKV + kvh) * SK * DH * 2;
    const char* vb = vc + (size_t)(b * HKV + kvh) * SK * DH * 2;
    const char* wqb = wq + (size_t)(kvh * 512 + half * 256) * 2;
    const char* wob = wo + (size_t)(kvh * 512 + half * 256) * 1024;
    const unsigned r4 = lane >> 4, c16 = lane & 15u;
    const unsigned swz = 16u * (c16 ^ (4u * hw + r4));
    const unsigned vo_k = (4u * hw + r4) * 256u + swz;
    const unsigned vo_x = (4u * hw + r4) * 1024u + swz;
    const unsigned r8 = (lane >> 2) & 7u, cb = 64u * (lane >> 5) + 16u * (lane & 3u);
    const unsigned vo_v = ((hw >> 1) * 8u + r8) * 256u + 128u * (hw & 1u) + cb;
    const unsigned vo_wq = r8 * 8192u + 128u * hw + cb;
    const unsigned vo_wo = (hw * 8u + r8) * 1024u + cb;
    const unsigned ld = ldsb + hw * 1024u;
#define DMA_X(c)  dma4(xb + (c) * 256, vo_x, 16u * 1024u, ld + x_slot(c))
#define DMA_WQ(i) dma4(wqb + (size_t)(i) * 32 * 8192, vo_wq, 8u * 8192u, ld + wq_slot(i))
#define DMA_K(t)  dma4(kb + (size_t)(t) * 16384, vo_k, 4096u, ld + ((t) & 3) * SLOT)
#define DMA_V(t)  dma4(vb + (size_t)(t) * 16384, vo_v, 4096u, ld + (4u + ((t) & 3)) * SLOT)
#define DMA_WO(n) dma4(wob + (size_t)((n) & 1) * 128 * 1024 + ((n) >> 1) * 128, vo_wo, 32u * 1024u, ld + wo_slot(n))
    DMA_X(0); DMA_WQ(0); DMA_WQ(1); DMA_WQ(2); DMA_WQ(3); DMA_X(1); DMA_K(0); DMA_X(2); DMA_X(3); DMA_K(1);
    VMW(32); HBAR();
    VMW(28); HBAR(); DMA_WQ(4);
    VMW(28); HBAR(); DMA_WQ(5);
    VMW(28); HBAR(); DMA_WQ(6);
    VMW(8);  HBAR(); DMA_WQ(7); DMA_V(0);
    VMW(12); HBAR(); DMA_WQ(8);
    VMW(12); HBAR(); DMA_WQ(9);
    VMW(12); HBAR(); DMA_WQ(10);
    VMW(8);  HBAR(); DMA_WQ(11); DMA_K(2);
    VMW(12); HBAR(); DMA_WQ(12);
    VMW(12); HBAR(); DMA_WQ(13);
    VMW(12); HBAR(); DMA_WQ(14);
    VMW(8);  HBAR(); DMA_WQ(15); DMA_V(1);
    VMW(12); HBAR();
    VMW(8);  HBAR();
    VMW(4);  HBAR();
    HBAR(); DMA_K(3); DMA_V(2); DMA_WO(0); DMA_WO(1);
    VMW(16); HBAR(); DMA_K(4); DMA_V(3);
    VMW(24); HBAR(); DMA_K(5); DMA_V(4);
    VMW(24); HBAR(); DMA_K(6); DMA_V(5);
    for (int t = 4; t <= 12; ++t) {
      VMW(16); HBAR(); DMA_K(t + 3); DMA_V(t + 2);
    }
    VMW(16); HBAR(); DMA_WO(2); DMA_V(15);
    VMW(16); HBAR(); DMA_WO(3); DMA_WO(4);
    VMW(16); HBAR(); DMA_WO(5);
    VMW(12); HBAR(); DMA_WO(6);
    HBAR(); DMA_WO(7);
    HBAR();
    VMW(24); HBAR();
    VMW(16); HBAR(); DMA_WO(8); DMA_WO(9);
    VMW(16); HBAR(); DMA_WO(10); DMA_WO(11);
    VMW(16); HBAR(); DMA_WO(12); DMA_WO(13);
    VMW(16); HBAR(); DMA_WO(14); DMA_WO(15);
    VMW(16); HBAR();
    VMW(8);  HBAR();
    VMW(0);  HBAR();
    return;
  }

  const unsigned hh = wid >> 1, qh = wid & 1u, l31 = lane & 31u, h = lane >> 5;
  unsigned ka[8];
#pragma unroll
  for (int s = 0; s < 8; ++s) ka[s] = ldsb + l31 * 256u + 16u * ((2u * s + h) ^ (lane & 15u));
  const unsigned trq = (lane >> 2) & 3u, trp = lane & 3u, trb = (lane >> 4) & 1u;
  const unsigned tr_nat = ldsb + 64u * trq + 32u * trb + 8u * (((trp & 1u) << 1) | (trp >> 1));
  const unsigned tr_v = ldsb + 64u * trq + 32u * trb + 8u * trp + 256u * h;

  f32x16 qacc[4] = {};
  {
    const unsigned wa = tr_nat + h * 4096u + hh * 2048u;
#pragma unroll
    for (int i = 0; i < 16; ++i) {
      BAR();
#pragma unroll
      for (int s = 0; s < 2; ++s) {
        const bf16x8 xf = ldsb128(ka[2 * (i & 3) + s] + x_slot(i >> 2) + qh * 8192u);
#pragma unroll
        for (int db = 0; db < 4; ++db) {
          const unsigned a = wa + wq_slot(i) + s * 8192u + db * 512u;
          qacc[db] = mfma(ldtr2(a, a + 256u), xf, qacc[db]);
        }
      }
    }
  }
  bf16x8 qf[8];
#pragma unroll
  for (int db = 0; db < 4; ++db)
#pragma unroll
    for (int u = 0; u < 2; ++u) {
      u32x4 w;
#pragma unroll
      for (int e = 0; e < 4; ++e) w[e] = cvtpk(qacc[db][8 * u + 2 * e] * QSCALE, qacc[db][8 * u + 2 * e + 1] * QSCALE);
      qf[2 * db + u] = __builtin_bit_cast(bf16x8, w);
    }

  f32x16 o[4] = {};
  float m_reg, l_reg = 0.f;
  f32x16 pA0, pA1, pB0, pB1;
  bf16x8 pa0, pa1, pa2, pa3;
  float alpha;

  auto qkt = [&](f32x16& p0, f32x16& p1, unsigned kslot) {
    p0 = f32x16{}; p1 = f32x16{};
#pragma unroll
    for (int s = 0; s < 8; ++s) {
      const unsigned a = ka[s] + kslot;
      p0 = mfma(ldsb128(a), qf[s], p0);
      p1 = mfma(ldsb128(a + 8192u), qf[s], p1);
    }
  };
  auto rowmax = [&](const f32x16& p0, const f32x16& p1) {
    float mx = fmaxf(p0[0], p1[0]);
#pragma unroll
    for (int r = 1; r < 16; ++r) mx = fmaxf(mx, fmaxf(p0[r], p1[r]));
    auto rr = __builtin_amdgcn_permlane32_swap(__float_as_uint(mx), __float_as_uint(mx), false, false);
    return fmaxf(__uint_as_float(rr[0]), __uint_as_float(rr[1]));
  };
  auto partialSM = [&](f32x16& p0, f32x16& p1) {
    const float pmax = rowmax(p0, p1);
    if (__builtin_expect(__all(pmax - m_reg <= THR), 1)) { alpha = 1.f; }
    else { const float mn = fmaxf(m_reg, pmax); alpha = __builtin_amdgcn_exp2f(m_reg - mn); m_reg = mn; }
#pragma unroll
    for (int r = 0; r < 16; ++r) { p0[r] -= m_reg; p1[r] -= m_reg; }
#pragma unroll
    for (int r = 0; r < 16; ++r) p0[r] = __builtin_amdgcn_exp2f(p0[r]);
  };
  auto finishSM = [&](f32x16& p0, f32x16& p1) {
#pragma unroll
    for (int r = 0; r < 16; ++r) p1[r] = __builtin_amdgcn_exp2f(p1[r]);
    float ps = 0.f;
#pragma unroll
    for (int r = 0; r < 16; ++r) ps += p0[r] + p1[r];
    l_reg = l_reg * alpha + ps;
#define PK8(P, BASE) __builtin_bit_cast(bf16x8, (u32x4){cvtpk(P[BASE], P[BASE + 1]), cvtpk(P[BASE + 2], P[BASE + 3]), cvtpk(P[BASE + 4], P[BASE + 5]), cvtpk(P[BASE + 6], P[BASE + 7])})
    pa0 = PK8(p0, 0); pa1 = PK8(p0, 8); pa2 = PK8(p1, 0); pa3 = PK8(p1, 8);
#undef PK8
  };
  auto pv = [&](unsigned vslot) {
    const unsigned a = tr_v + vslot;
#pragma unroll
    for (int db = 0; db < 4; ++db) {
      o[db] = mfma(ldtr2(a + db * 512u,           a + db * 512u + 2048u),         pa0, o[db]);
      o[db] = mfma(ldtr2(a + db * 512u + 4096u,   a + db * 512u + 4096u + 2048u), pa1, o[db]);
      o[db] = mfma(ldtr2(a + db * 512u + 8192u,   a + db * 512u + 8192u + 2048u), pa2, o[db]);
      o[db] = mfma(ldtr2(a + db * 512u + 12288u,  a + db * 512u + 12288u + 2048u), pa3, o[db]);
    }
  };
  auto resc = [&]() {
    if (__any(alpha < 1.f)) {
#pragma unroll
      for (int db = 0; db < 4; ++db)
#pragma unroll
        for (int r = 0; r < 16; ++r) o[db][r] *= alpha;
    }
  };
  BAR();
  qkt(pA0, pA1, 0u);
  m_reg = rowmax(pA0, pA1); alpha = 1.f;
#pragma unroll
  for (int r = 0; r < 16; ++r) { pA0[r] -= m_reg; pA1[r] -= m_reg; }
#pragma unroll
  for (int r = 0; r < 16; ++r) pA0[r] = __builtin_amdgcn_exp2f(pA0[r]);
  for (int t = 1; t < 15; t += 2) {
    BAR();
    qkt(pB0, pB1, (unsigned)(t & 3) * SLOT);
    finishSM(pA0, pA1);
    SBAR();
    pv((4u + (unsigned)((t - 1) & 3)) * SLOT);
    partialSM(pB0, pB1);
    resc();
    BAR();
    qkt(pA0, pA1, (unsigned)((t + 1) & 3) * SLOT);
    finishSM(pB0, pB1);
    SBAR();
    pv((4u + (unsigned)(t & 3)) * SLOT);
    partialSM(pA0, pA1);
    resc();
  }
  BAR();
  qkt(pB0, pB1, 3u * SLOT);
  finishSM(pA0, pA1);
  SBAR();
  pv((4u + 2u) * SLOT);
  partialSM(pB0, pB1);
  resc();
  BAR();
  finishSM(pB0, pB1);
  pv((4u + 3u) * SLOT);

  {
    auto rr = __builtin_amdgcn_permlane32_swap(__float_as_uint(l_reg), __float_as_uint(l_reg), false, false);
    l_reg = __uint_as_float(rr[0]) + __uint_as_float(rr[1]);
  }
  const float il = 1.0f / l_reg;
  bf16x8 of[16];
  BAR();
  {
    const unsigned xw = ldsb + 2u * SLOT + wid * 8192u + lane * 16u;
    const unsigned xr = ldsb + 2u * SLOT + (wid ^ 2u) * 8192u + lane * 16u;
    bf16x8 own[8];
#pragma unroll
    for (int db = 0; db < 4; ++db)
#pragma unroll
      for (int u = 0; u < 2; ++u) {
        u32x4 w;
#pragma unroll
        for (int e = 0; e < 4; ++e) w[e] = cvtpk(o[db][8 * u + 2 * e] * il, o[db][8 * u + 2 * e + 1] * il);
        own[2 * db + u] = __builtin_bit_cast(bf16x8, w);
        *(LAS bf16x8*)(uintptr_t)(xw + (2 * db + u) * 1024u) = own[2 * db + u];
      }
    BAR();
#pragma unroll
    for (int s = 0; s < 8; ++s) {
      const bf16x8 oth = ldsb128(xr + s * 1024u);
      of[s] = hh == 0 ? own[s] : oth;
      of[8 + s] = hh == 0 ? oth : own[s];
    }
  }
  {
    const unsigned wa = tr_v + hh * 512u;
    char* sl = slab + ((size_t)blockIdx.x * 32 + wid) * 4096 + lane * 16u;
#pragma unroll
    for (int c = 0; c < 8; ++c) {
      BAR();
      f32x16 acc = {};
#pragma unroll
      for (int s = 0; s < 16; ++s) {
        const unsigned a = wa + wo_slot(2 * c + (s >> 3)) + (s & 7) * 2048u;
        acc = mfma(ldtr2(a, a + 1024u), of[s], acc);
      }
#pragma unroll
      for (int g = 0; g < 4; ++g)
        *(f32x4*)(sl + (size_t)c * 4 * 4096 + g * 1024) = (f32x4){acc[4 * g], acc[4 * g + 1], acc[4 * g + 2], acc[4 * g + 3]};
    }
  }
}

__global__ __launch_bounds__(512) void reduce_kernel(const char* __restrict__ slab, unsigned short* __restrict__ out) {
  const unsigned b = blockIdx.x >> 4, j = blockIdx.x & 15u;
  const unsigned t = threadIdx.x, lane = t & 63u, g = (t >> 6) & 3u, f = t >> 8;
  const unsigned frag = (j >> 1) * 4 + 2 * (j & 1u) + f;
  f32x4 s = {0.f, 0.f, 0.f, 0.f};
#pragma unroll
  for (int p = 0; p < 16; ++p) {
    const f32x4 v = *(const f32x4*)(slab + ((size_t)(b * 16 + p) * 32 + frag) * 4096 + g * 1024 + lane * 16);
    s += v;
  }
  const unsigned q = 32 * f + (lane & 31u), n = 32 * j + 8 * g + 4 * (lane >> 5);
  u32x2 w = {cvtpk(s[0], s[1]), cvtpk(s[2], s[3])};
  *(u32x2*)(out + ((size_t)(b * SQ + q)) * DM + n) = w;
}

extern "C" void kernel_launch(void* const* d_in, const int* in_sizes, int n_in, void* d_out, int out_size, void* d_ws, size_t ws_size, hipStream_t stream) {
  static int ok = 0;
  if (!ok) { (void)hipFuncSetAttribute((const void*)fused_kernel, hipFuncAttributeMaxDynamicSharedMemorySize, (int)LDS_BYTES); ok = 1; }
  hipLaunchKernelGGL(fused_kernel, dim3(256), dim3(512), LDS_BYTES, stream, (const char*)d_in[0], (const char*)d_in[1], (const char*)d_in[2],
                     (const char*)d_in[3], (const char*)d_in[4], (char*)d_ws);
  hipLaunchKernelGGL(reduce_kernel, dim3(256), dim3(512), 0, stream, (const char*)d_ws, (unsigned short*)d_out);
}
```

```cpp
#include <hip/hip_runtime.h>
#include <stdint.h>
#include <utility>

typedef __bf16 bf16x8 __attribute__((ext_vector_type(8)));
typedef __bf16 bf16x4 __attribute__((ext_vector_type(4)));
typedef __bf16 bf16x2 __attribute__((ext_vector_type(2)));
typedef float f32x16 __attribute__((ext_vector_type(16)));
typedef float f32x4 __attribute__((ext_vector_type(4)));
typedef float f32x2 __attribute__((ext_vector_type(2)));
typedef unsigned u32x4 __attribute__((ext_vector_type(4)));
typedef unsigned u32x2 __attribute__((ext_vector_type(2)));
#define LAS __attribute__((address_space(3)))
#define GAS __attribute__((address_space(1)))
#define DI __device__ __forceinline__
template <int... Is> using iseq = std::integer_sequence<int, Is...>;
template <int N> using mkseq = std::make_integer_sequence<int, N>;

constexpr int NB = 16, SQ = 64, DM = 512, HQ = 32, HKV = 8, SK = 1024, DH = 128;
constexpr unsigned SLOT = 16384u;
constexpr unsigned LDS_BYTES = 10 * SLOT;
constexpr float QSCALE = 0.12751743082f;
constexpr float THR = 8.0f;
#define KV_AUX 2
#define W_AUX 0

#define BARL() asm volatile("s_waitcnt lgkmcnt(0)\n\ts_barrier" ::: "memory")
#define BAR() asm volatile("s_barrier" ::: "memory")
#define VMW(n) asm volatile("s_waitcnt vmcnt(%0)" :: "n"(n) : "memory")
#define SB() __builtin_amdgcn_sched_barrier(0)
#define TS(i) do {} while (0)
#define TSH(i) do {} while (0)

DI unsigned cvtpk(float lo, float hi) { f32x2 v = {lo, hi}; bf16x2 b = __builtin_convertvector(v, bf16x2); return __builtin_bit_cast(unsigned, b); }
DI bf16x8 ldsb128(unsigned a) { return *(const LAS bf16x8*)(uintptr_t)a; }
DI bf16x8 ldtr2(unsigned a0, unsigned a1) {
  bf16x4 lo = __builtin_amdgcn_ds_read_tr16_b64_v4bf16((LAS bf16x4*)(uintptr_t)a0);
  bf16x4 hi = __builtin_amdgcn_ds_read_tr16_b64_v4bf16((LAS bf16x4*)(uintptr_t)a1);
  return __builtin_shufflevector(lo, hi, 0, 1, 2, 3, 4, 5, 6, 7);
}
DI f32x16 mfma(bf16x8 a, bf16x8 b, f32x16 c) { return __builtin_amdgcn_mfma_f32_32x32x16_bf16(a, b, c, 0, 0, 0); }
DI float mx3(float a, float b, float c) { return __builtin_fmaxf(__builtin_fmaxf(a, b), c); }

template <int AUX> DI void dma8(const char* g, unsigned voA, unsigned voB, unsigned gpair, unsigned godd, unsigned ldsaddr) {
#pragma unroll
  for (int i = 0; i < 8; ++i)
    __builtin_amdgcn_global_load_lds((const GAS void*)(g + (size_t)(i >> 1) * gpair + (size_t)(i & 1) * godd + ((i & 1) ? voB : voA)), (LAS void*)(uintptr_t)(ldsaddr + i * 2048u), 16, 0, AUX);
}

constexpr unsigned wq_slot(int i) { return (6u + (unsigned)(i & 3)) * SLOT; }
constexpr unsigned x_slot(int c) { return (c == 0 ? 4u : c == 1 ? 2u : c == 2 ? 5u : 3u) * SLOT; }
constexpr unsigned wo_slot(int n) { return (n == 0 ? 0u : n == 1 ? 8u : n == 2 ? 1u : n == 3 ? 9u : n == 4 ? 2u : n == 5 ? 4u : n == 6 ? 3u : n == 7 ? 5u :
                                            n == 8 ? 6u : n == 9 ? 7u : n == 10 ? 0u : n == 11 ? 8u : n == 12 ? 1u : n == 13 ? 9u : n == 14 ? 2u : 4u) * SLOT; }

struct CW {
  f32x16 o[4];
  bf16x8 qf[8];
  f32x16 S[2][2];
  f32x16 negm;
  bf16x8 kf[16], vf[16];
  u32x4 pa[4];
  unsigned ka[8];
  float l, ps, mx;
};

constexpr int first_e(int g) { return g <= 12 ? g + g / 2 : (g + 6 > 32 ? 32 : g + 6); }
template <int Y, int E> DI void sm_exp(CW& c) { c.S[Y][E >> 4][E & 15] = __builtin_amdgcn_exp2f(c.S[Y][E >> 4][E & 15]); }
template <int Y, int E> DI void sm_add(CW& c) { c.ps += c.S[Y][E >> 4][E & 15]; }
template <int Y, int C> DI void sm_cvt(CW& c) { c.pa[C >> 2][C & 3] = cvtpk(c.S[Y][C >> 3][(2 * C) & 15], c.S[Y][C >> 3][(2 * C + 1) & 15]); }
template <int X, int I> DI void sm_max(CW& c) {
  c.mx = mx3(c.mx, c.S[X][I >> 3][(2 * I) & 15], c.S[X][I >> 3][(2 * I + 1) & 15]);
}
template <int T4, bool QK, bool PV, bool FIRST, bool NEXTK, int G> DI void att_gap(CW& c, const unsigned vcur) {
  constexpr int X = T4 & 1, Y = X ^ 1;
  if constexpr (G == 28 && QK) { BAR(); }
  if constexpr (G < 16) {
    if constexpr (QK) {
      constexpr int kb = G >> 3, s = G & 7;
      if constexpr (s == 0) {
        if constexpr (FIRST) c.S[X][kb] = mfma(c.kf[G], c.qf[0], f32x16{});
        else asm volatile("v_mfma_f32_32x32x16_bf16 %0, %1, %2, %3" : "=&v"(c.S[X][kb]) : "v"(c.kf[G]), "v"(c.qf[0]), "v"(c.negm));
      } else c.S[X][kb] = mfma(c.kf[G], c.qf[s], c.S[X][kb]);
    }
  } else if constexpr (PV) {
    constexpr int j = G - 16, ks = j >> 2, db = j & 3;
    c.o[db] = mfma(c.vf[j], __builtin_bit_cast(bf16x8, c.pa[ks]), c.o[db]);
  }
  SB();
  if constexpr (G < 12 && QK) { constexpr int n = G + 4; c.kf[n] = ldsb128(c.ka[n & 7] + (unsigned)T4 * SLOT + (n >> 3) * 8192u); }
  if constexpr (G >= 28 && NEXTK) { constexpr int n = G - 28; c.kf[n] = ldsb128(c.ka[n] + (unsigned)((T4 + 1) & 3) * SLOT); }
  if constexpr (G >= 12 && G < 28 && PV) { constexpr int j = G - 12, ks = j >> 2, db = j & 3; const unsigned a = vcur + db * 512u + ks * 4096u; c.vf[j] = ldtr2(a, a + 2048u); }
  if constexpr (PV) {
    constexpr int e0 = first_e(G), e1 = first_e(G + 1), p0 = G ? first_e(G - 1) : 0;
    if constexpr (G == 0) c.ps = 0.f;
    if constexpr (e0 < e1) sm_exp<Y, e0>(c);
    if constexpr (p0 < e0) sm_add<Y, p0>(c);
    if constexpr (e0 + 1 < e1) sm_exp<Y, e0 + 1>(c);
    if constexpr (p0 + 1 < e0) sm_add<Y, p0 + 1>(c);
    if constexpr (p0 < e0 && (p0 & 1)) sm_cvt<Y, (p0 >> 1)>(c);
    if constexpr (p0 + 1 < e0 && ((p0 + 1) & 1)) sm_cvt<Y, ((p0 + 1) >> 1)>(c);
    if constexpr (G == 27) { c.l += c.ps; }
    if constexpr (G <= 27) asm volatile("" : "+v"(c.ps), "+v"(c.l));
  }
  if constexpr (QK) {
    if constexpr (G == 12) c.mx = FIRST ? -3.0e38f : 0.f;
    if constexpr (G >= 13 && G <= 25 && (G & 1)) sm_max<X, (G - 13) / 2>(c);
    if constexpr (G == 27) { sm_max<X, 7>(c); sm_max<X, 8>(c); }
    if constexpr (G == 28) { sm_max<X, 9>(c); sm_max<X, 10>(c); sm_max<X, 11>(c); }
    if constexpr (G == 29) { sm_max<X, 12>(c); sm_max<X, 13>(c); sm_max<X, 14>(c); }
    if constexpr (G == 30) {
      sm_max<X, 15>(c);
      auto rr = __builtin_amdgcn_permlane32_swap(__float_as_uint(c.mx), __float_as_uint(c.mx), false, false);
      c.mx = mx3(__uint_as_float(rr[0]), __uint_as_float(rr[1]), c.mx);
    }
    if constexpr (G >= 13) asm volatile("" : "+v"(c.mx));
  }
  SB();
}
template <int T4, bool QK, bool PV, bool FIRST, bool NEXTK, int... Gs> DI void att_period_(CW& c, const unsigned vcur, iseq<Gs...>) {
  constexpr int X = T4 & 1;
  (att_gap<T4, QK, PV, FIRST, NEXTK, Gs>(c, vcur), ...);
  asm volatile("" ::: "memory");
  if constexpr (FIRST) {
#pragma unroll
    for (int kb = 0; kb < 2; ++kb)
#pragma unroll
      for (int r = 0; r < 16; ++r) c.S[X][kb][r] -= c.mx;
#pragma unroll
    for (int r = 0; r < 16; ++r) c.negm[r] = -c.mx;
    asm volatile("s_nop 1" : "+v"(c.negm));
  } else if constexpr (QK) {
    if (__builtin_expect(__any(c.mx > THR), 0)) {
      const float dl = c.mx, f = __builtin_amdgcn_exp2f(-dl);
#pragma unroll
      for (int kb = 0; kb < 2; ++kb)
#pragma unroll
        for (int r = 0; r < 16; ++r) c.S[X][kb][r] -= dl;
#pragma unroll
      for (int db = 0; db < 4; ++db)
#pragma unroll
        for (int r = 0; r < 16; ++r) c.o[db][r] *= f;
      c.l *= f;
#pragma unroll
      for (int r = 0; r < 16; ++r) c.negm[r] -= dl;
      asm volatile("s_nop 1" : "+v"(c.negm));
    }
  }
  SB();
}
template <int T4, bool QK, bool PV, bool FIRST, bool NEXTK> DI void att_period(CW& c, const unsigned vcur) { att_period_<T4, QK, PV, FIRST, NEXTK>(c, vcur, mkseq<32>{}); }

struct QW { f32x16 acc[4]; bf16x8 a[2][8]; bf16x8 xf[2][2]; };
template <int I, int G> DI void q_gap(QW& q, const CW& c, const unsigned wa, const unsigned xoff) {
  constexpr int P = (I - 1) & 1, N = I & 1;
  if constexpr (I > 0) { constexpr int s = G >> 2, db = G & 3; q.acc[db] = mfma(q.a[P][G], q.xf[P][s], q.acc[db]); }
  SB();
  if constexpr (I < 16) {
    if constexpr (G == 0) q.xf[N][0] = ldsb128(c.ka[2 * (I & 3)] + x_slot(I >> 2) + xoff);
    if constexpr (G == 3) q.xf[N][1] = ldsb128(c.ka[2 * (I & 3) + 1] + x_slot(I >> 2) + xoff);
    if constexpr (G < 6) {
      constexpr int f0 = (8 * G) / 6, f1 = (8 * (G + 1)) / 6;
      if constexpr (f0 < f1) { constexpr int f = f0; const unsigned a = wa + wq_slot(I) + (f >> 2) * 8192u + (f & 3) * 512u; q.a[N][f] = ldtr2(a, a + 256u); }
      if constexpr (f0 + 1 < f1) { constexpr int f = f0 + 1; const unsigned a = wa + wq_slot(I) + (f >> 2) * 8192u + (f & 3) * 512u; q.a[N][f] = ldtr2(a, a + 256u); }
    }
  }
  SB();
}
template <int I, int... Gs> DI void q_step_(QW& q, const CW& c, const unsigned wa, const unsigned xoff, iseq<Gs...>) { (q_gap<I, Gs>(q, c, wa, xoff), ...); }
template <int I> DI void q_step(QW& q, const CW& c, const unsigned wa, const unsigned xoff) { BARL(); q_step_<I>(q, c, wa, xoff, mkseq<8>{}); }
template <int... Is> DI void q_steps(QW& q, const CW& c, const unsigned wa, const unsigned xoff, iseq<Is...>) { (q_step<Is>(q, c, wa, xoff), ...); }

struct OW { bf16x8 of[16]; bf16x8 a[2][16]; f32x16 acc[2]; };
template <int C, int G> DI void o_gap(OW& w, const unsigned wa, char* sl, const unsigned rot) {
  constexpr int P = (C - 1) & 1, N = C & 1;
  if constexpr (C > 0 && C <= 8) { if constexpr (G == 0) w.acc[P] = mfma(w.a[P][0], w.of[0], f32x16{}); else w.acc[P] = mfma(w.a[P][G], w.of[G], w.acc[P]); }
  SB();
  if constexpr (C < 8 && G < 14) {
    constexpr int f0 = (16 * G) / 14, f1 = (16 * (G + 1)) / 14;
    if constexpr (f0 < f1) { constexpr int f = f0; const unsigned a = wa + wo_slot(2 * C + (f >> 3)) + (f & 7) * 2048u; w.a[N][f] = ldtr2(a, a + 1024u); }
    if constexpr (f0 + 1 < f1) { constexpr int f = f0 + 1; const unsigned a = wa + wo_slot(2 * C + (f >> 3)) + (f & 7) * 2048u; w.a[N][f] = ldtr2(a, a + 1024u); }
  }
  if constexpr (C >= 2 && G >= 4 && G < 8) {
    constexpr int g = G - 4;
    const unsigned cc = ((unsigned)(C - 2) + rot) & 7u;
    *(f32x4*)(sl + (size_t)cc * 4 * 4096 + g * 1024) = (f32x4){w.acc[N][4 * g], w.acc[N][4 * g + 1], w.acc[N][4 * g + 2], w.acc[N][4 * g + 3]};
  }
  SB();
}
template <int C, int... Gs> DI void o_step_(OW& w, const unsigned wa, char* sl, const unsigned rot, iseq<Gs...>) { (o_gap<C, Gs>(w, wa, sl, rot), ...); }

__global__ __launch_bounds__(512, 2) void fused_kernel(const char* __restrict__ x, const char* __restrict__ kc, const char* __restrict__ vc,
                                                       const char* __restrict__ wq, const char* __restrict__ wo, char* __restrict__ slab) {
  extern __shared__ __attribute__((aligned(16))) char lds[];
  const unsigned ldsb = (unsigned)(uintptr_t)lds;
  const unsigned lane = threadIdx.x & 63u;
  const unsigned wid = __builtin_amdgcn_readfirstlane(threadIdx.x >> 6);
  const unsigned kvh = blockIdx.x & 7u, half = (blockIdx.x >> 3) & 1u, b = blockIdx.x >> 4;

  if (wid >= 4) {
    const unsigned hw2 = wid & 1u;
    const unsigned r4 = lane >> 4, c16 = lane & 15u;
    const unsigned swz = 16u * (c16 ^ (4u * hw2 + r4));
    const unsigned r8 = (lane >> 2) & 7u, cb = 64u * (lane >> 5) + 16u * (lane & 3u);
    const unsigned ld = ldsb + hw2 * 1024u;
    if (wid >= 6) {
      const char* kb = kc + (size_t)(b * HKV + kvh) * SK * DH * 2;
      const char* vb = vc + (size_t)(b * HKV + kvh) * SK * DH * 2;
      const unsigned vo_kA = (4u * hw2 + r4) * 256u + swz, vo_kB = vo_kA ^ 128u;
      const unsigned vo_v = r8 * 256u + 128u * hw2 + cb;
#define DMA_K(t)  dma8<KV_AUX>(kb + (size_t)((t) ^ half) * 16384, vo_kA, vo_kB, 4096u, 2048u, ld + ((t) & 3) * SLOT)
#define DMA_V(t)  dma8<KV_AUX>(vb + (size_t)((t) ^ half) * 16384, vo_v, vo_v, 4096u, 2048u, ld + (4u + ((t) % 6u)) * SLOT)
      TSH(26);
      DMA_K(0u); DMA_K(1u);
      TSH(28);
      BAR(); BAR(); BAR(); BAR();
      BAR(); DMA_V(0u);
      BAR(); BAR(); BAR();
      BAR(); DMA_K(2u);
      BAR(); BAR(); BAR();
      BAR(); DMA_V(1u);
      BAR(); DMA_V(2u);
      BAR(); BAR();
      for (unsigned u = 0; u <= 12; ++u) { VMW(40); BAR(); DMA_K(u + 3u); DMA_V(u + 3u); }
      VMW(40); BAR();
      VMW(24); BAR();
      VMW(8);  BAR();
      VMW(0);  BAR();
      BAR();
      BAR(); BAR(); BAR(); BAR(); BAR(); BAR(); BAR(); BAR();
      return;
    }
    const unsigned rq = b & 3u, ro = b & 7u;
    const char* xb = x + (size_t)b * SQ * DM * 2;
    const char* wqb = wq + (size_t)(kvh * 512 + half * 256) * 2;
    const char* wob = wo + (size_t)(kvh * 512 + half * 256) * 1024;
    const unsigned vo_xA = (4u * hw2 + r4) * 1024u + swz, vo_xB = vo_xA ^ 128u;
    const unsigned vo_wq = r8 * 8192u + 128u * hw2 + cb;
    const unsigned vo_wo = (hw2 * 8u + r8) * 1024u + cb;
#define DMA_X(c)  dma8<W_AUX>(xb + (((c) + rq) & 3u) * 256u, vo_xA, vo_xB, 16u * 1024u, 8u * 1024u, ld + x_slot(c))
#define DMA_WQ(i) dma8<W_AUX>(wqb + (size_t)((4u * ((((i) >> 2) + rq) & 3u) + ((i) & 3)) * 32u) * 8192u, vo_wq, vo_wq, 8u * 8192u, 256u, ld + wq_slot(i))
#define DMA_WO(n) dma8<W_AUX>(wob + (size_t)((n) & 1) * 128 * 1024 + ((((n) >> 1) + ro) & 7u) * 128u, vo_wo, vo_wo, 32u * 1024u, 16u * 1024u, ld + wo_slot(n))
    TSH(26);
    DMA_X(0); DMA_WQ(0); DMA_WQ(1); DMA_WQ(2); DMA_WQ(3); DMA_X(1); DMA_X(2);
    TSH(28);
    VMW(40); BAR();
    TSH(30);
    VMW(32); BAR(); DMA_WQ(4); DMA_X(3);
    VMW(40); BAR(); DMA_WQ(5);
    VMW(40); BAR(); DMA_WQ(6);
    VMW(24); BAR(); DMA_WQ(7);
    VMW(16); BAR(); DMA_WQ(8);
    VMW(16); BAR(); DMA_WQ(9);
    VMW(16); BAR(); DMA_WQ(10);
    VMW(16); BAR(); DMA_WQ(11);
    VMW(16); BAR(); DMA_WQ(12);
    VMW(16); BAR(); DMA_WQ(13);
    VMW(16); BAR(); DMA_WQ(14);
    VMW(16); BAR(); DMA_WQ(15);
    VMW(16); BAR();
    VMW(8);  BAR();
    VMW(0);  BAR();
    for (int u = 0; u <= 12; ++u) BAR();
    BAR(); DMA_WO(0); DMA_WO(1);
    BAR(); DMA_WO(2); DMA_WO(3);
    BAR(); DMA_WO(4); DMA_WO(5);
    BAR(); DMA_WO(6); DMA_WO(7);
    BAR();
    VMW(48); BAR();
    VMW(32); BAR(); DMA_WO(8); DMA_WO(9); DMA_WO(10); DMA_WO(11);
    VMW(48); BAR(); DMA_WO(12); DMA_WO(13);
    VMW(48); BAR(); DMA_WO(14); DMA_WO(15);
    VMW(48); BAR();
    VMW(32); BAR();
    VMW(16); BAR();
    VMW(0);  BAR();
    return;
  }

  TS(0);
  const unsigned hh = wid >> 1, qh = wid & 1u, l31 = lane & 31u, h = lane >> 5;
  CW c;
#pragma unroll
  for (int s = 0; s < 8; ++s) c.ka[s] = ldsb + l31 * 256u + 16u * ((2u * s + h) ^ (lane & 15u));
  const unsigned trq = (lane >> 2) & 3u, trp = lane & 3u, trb = (lane >> 4) & 1u;
  const unsigned tr_nat = ldsb + 64u * trq + 32u * trb + 8u * (((trp & 1u) << 1) | (trp >> 1));
  const unsigned tr_v = ldsb + 64u * trq + 32u * trb + 8u * trp + 256u * h;

  {
    QW q;
#pragma unroll
    for (int db = 0; db < 4; ++db) q.acc[db] = f32x16{};
    const unsigned wa = tr_nat + h * 4096u + hh * 2048u;
    q_steps(q, c, wa, qh * 8192u, mkseq<16>{});
    TS(4);
    BARL();
    q_step_<16>(q, c, wa, qh * 8192u, mkseq<8>{});
#pragma unroll
    for (int n = 0; n < 4; ++n) c.kf[n] = ldsb128(c.ka[n]);
#pragma unroll
    for (int db = 0; db < 4; ++db)
#pragma unroll
      for (int u = 0; u < 2; ++u) {
        u32x4 w;
#pragma unroll
        for (int e = 0; e < 4; ++e) w[e] = cvtpk(q.acc[db][8 * u + 2 * e] * QSCALE, q.acc[db][8 * u + 2 * e + 1] * QSCALE);
        c.qf[2 * db + u] = __builtin_bit_cast(bf16x8, w);
      }
  }
  TS(5);
#pragma unroll
  for (int db = 0; db < 4; ++db) c.o[db] = f32x16{};
  c.l = 0.f; c.ps = 0.f; c.mx = 0.f;
  SB();
  att_period<0, true, false, true, true>(c, 0u);
  unsigned vslot = 0;
  auto vnext = [&]() { const unsigned a = tr_v + (4u + vslot) * SLOT; vslot = vslot == 5 ? 0 : vslot + 1; return a; };
  for (int t = 1; t < 13; t += 4) {
    att_period<1, true, true, false, true>(c, vnext());
    TS(6 + (t >> 2));
    att_period<2, true, true, false, true>(c, vnext());
    att_period<3, true, true, false, true>(c, vnext());
    att_period<0, true, true, false, true>(c, vnext());
  }
  TS(9);
  att_period<1, true, true, false, true>(c, vnext());
  att_period<2, true, true, false, true>(c, vnext());
  att_period<3, true, true, false, false>(c, vnext());
  TS(10);
  att_period<0, false, true, false, false>(c, vnext());
  TS(11);

  {
    auto rr = __builtin_amdgcn_permlane32_swap(__float_as_uint(c.l), __float_as_uint(c.l), false, false);
    c.l = __uint_as_float(rr[0]) + __uint_as_float(rr[1]);
  }
  const float il = 1.0f / c.l;
  OW w;
  BARL();
  {
    const unsigned xw = ldsb + 6u * SLOT + wid * 8192u + lane * 16u;
    const unsigned xr = ldsb + 6u * SLOT + (wid ^ 2u) * 8192u + lane * 16u;
    bf16x8 own[8];
#pragma unroll
    for (int db = 0; db < 4; ++db)
#pragma unroll
      for (int u = 0; u < 2; ++u) {
        u32x4 v;
#pragma unroll
        for (int e = 0; e < 4; ++e) v[e] = cvtpk(c.o[db][8 * u + 2 * e] * il, c.o[db][8 * u + 2 * e + 1] * il);
        own[2 * db + u] = __builtin_bit_cast(bf16x8, v);
        *(LAS bf16x8*)(uintptr_t)(xw + (2 * db + u) * 1024u) = own[2 * db + u];
      }
    BARL();
    TS(12);
#pragma unroll
    for (int s = 0; s < 8; ++s) {
      const bf16x8 oth = ldsb128(xr + s * 1024u);
      w.of[s] = hh == 0 ? own[s] : oth;
      w.of[8 + s] = hh == 0 ? oth : own[s];
    }
  }
  {
    const unsigned wa = tr_v + hh * 512u;
    const unsigned ro = b & 7u;
    char* sl = slab + ((size_t)blockIdx.x * 32 + wid) * 4096 + lane * 16u;
    o_step_<0>(w, wa, sl, ro, mkseq<16>{});
    BARL(); o_step_<1>(w, wa, sl, ro, mkseq<16>{});
    TS(13);
    BARL(); o_step_<2>(w, wa, sl, ro, mkseq<16>{});
    BARL(); o_step_<3>(w, wa, sl, ro, mkseq<16>{});
    BARL(); o_step_<4>(w, wa, sl, ro, mkseq<16>{});
    TS(14);
    BARL(); o_step_<5>(w, wa, sl, ro, mkseq<16>{});
    BARL(); o_step_<6>(w, wa, sl, ro, mkseq<16>{});
    BARL(); o_step_<7>(w, wa, sl, ro, mkseq<16>{});
    o_step_<8>(w, wa, sl, ro, mkseq<16>{});
    o_step_<9>(w, wa, sl, ro, mkseq<16>{});
  }
  TS(24);
}

__global__ __launch_bounds__(512) void reduce_kernel(const char* __restrict__ slab, unsigned short* __restrict__ out) {
  const unsigned b = blockIdx.x >> 4, j = blockIdx.x & 15u;
  const unsigned t = threadIdx.x, lane = t & 63u, g = (t >> 6) & 3u, f = t >> 8;
  const unsigned frag = (j >> 1) * 4 + 2 * (j & 1u) + f;
  f32x4 s = {0.f, 0.f, 0.f, 0.f};
#pragma unroll
  for (int p = 0; p < 16; ++p) {
    const f32x4 v = *(const f32x4*)(slab + ((size_t)(b * 16 + p) * 32 + frag) * 4096 + g * 1024 + lane * 16);
    s += v;
  }
  const unsigned q = 32 * f + (lane & 31u), n = 32 * j + 8 * g + 4 * (lane >> 5);
  u32x2 w = {cvtpk(s[0], s[1]), cvtpk(s[2], s[3])};
  *(u32x2*)(out + ((size_t)(b * SQ + q)) * DM + n) = w;
}

extern "C" void kernel_launch(void* const* d_in, const int* in_sizes, int n_in, void* d_out, int out_size, void* d_ws, size_t ws_size, hipStream_t stream) {
  static int ok = 0;
  if (!ok) { (void)hipFuncSetAttribute((const void*)fused_kernel, hipFuncAttributeMaxDynamicSharedMemorySize, (int)LDS_BYTES); ok = 1; }
  hipLaunchKernelGGL(fused_kernel, dim3(256), dim3(512), LDS_BYTES, stream, (const char*)d_in[0], (const char*)d_in[1], (const char*)d_in[2],
                     (const char*)d_in[3], (const char*)d_in[4], (char*)d_ws);
  hipLaunchKernelGGL(reduce_kernel, dim3(256), dim3(512), 0, stream, (const char*)d_ws, (unsigned short*)d_out);
}
```

```cpp
#include <hip/hip_runtime.h>
#include <stdint.h>
#include <utility>

typedef __bf16 bf16x8 __attribute__((ext_vector_type(8)));
typedef __bf16 bf16x4 __attribute__((ext_vector_type(4)));
typedef __bf16 bf16x2 __attribute__((ext_vector_type(2)));
typedef float f32x16 __attribute__((ext_vector_type(16)));
typedef float f32x4 __attribute__((ext_vector_type(4)));
typedef float f32x2 __attribute__((ext_vector_type(2)));
typedef unsigned u32x4 __attribute__((ext_vector_type(4)));
typedef unsigned u32x2 __attribute__((ext_vector_type(2)));
#define LAS __attribute__((address_space(3)))
#define GAS __attribute__((address_space(1)))
#define DI __device__ __forceinline__
template <int... Is> using iseq = std::integer_sequence<int, Is...>;
template <int N> using mkseq = std::make_integer_sequence<int, N>;

constexpr int NB = 16, SQ = 64, DM = 512, HQ = 32, HKV = 8, SK = 1024, DH = 128;
constexpr unsigned SLOT = 16384u;
constexpr unsigned LDS_BYTES = 10 * SLOT;
constexpr float QSCALE = 0.12751743082f;
constexpr float THR = 8.0f;
constexpr unsigned WS_EPOCH = 0;
constexpr unsigned WS_FLAG = 4096;
constexpr unsigned WS_SLAB = 65536;
constexpr unsigned FRAG_B = 2048;
constexpr unsigned SLAB_WG = 36 * FRAG_B;
#define KV_NT true
#define W_NT false

#define BAR() asm volatile("s_barrier" ::: "memory")
#define LGKM(n) __builtin_amdgcn_s_waitcnt(0xC07F | ((n) << 8))
#define BARL() do { LGKM(0); BAR(); } while (0)
#define VMW(n) asm volatile("s_waitcnt vmcnt(%0)" :: "n"(n) : "memory")
#define SB() __builtin_amdgcn_sched_barrier(0)
#define TS(i) do {} while (0)
#define TSH(i) do {} while (0)

DI unsigned cvtpk(float lo, float hi) { f32x2 v = {lo, hi}; bf16x2 b = __builtin_convertvector(v, bf16x2); return __builtin_bit_cast(unsigned, b); }
DI bf16x8 ldsb128(unsigned a) { return *(const LAS bf16x8*)(uintptr_t)a; }
DI bf16x8 ldtr2(unsigned a0, unsigned a1) {
  bf16x4 lo = __builtin_amdgcn_ds_read_tr16_b64_v4bf16((LAS bf16x4*)(uintptr_t)a0);
  bf16x4 hi = __builtin_amdgcn_ds_read_tr16_b64_v4bf16((LAS bf16x4*)(uintptr_t)a1);
  return __builtin_shufflevector(lo, hi, 0, 1, 2, 3, 4, 5, 6, 7);
}
DI f32x16 mfma(bf16x8 a, bf16x8 b, f32x16 c) { return __builtin_amdgcn_mfma_f32_32x32x16_bf16(a, b, c, 0, 0, 0); }
DI float mx3(float a, float b, float c) { return __builtin_fmaxf(__builtin_fmaxf(a, b), c); }

template <bool NT, int MSTEP> DI void dma4(__amdgpu_buffer_rsrc_t srd, unsigned vo, unsigned so, unsigned d1, unsigned d2, unsigned lds) {
  unsigned s1, s2;
#define DMA4_BODY(NTS) \
  asm volatile( \
    "s_mov_b32 m0, %[lds]\n\t" \
    "s_add_u32 %[s1], %[so], %[d1]\n\t" \
    "buffer_load_dwordx4 %[vo], %[srd], %[so] offen" NTS " lds\n\t" \
    "s_add_u32 m0, m0, %[ms]\n\t" \
    "s_add_u32 %[s2], %[so], %[d2]\n\t" \
    "buffer_load_dwordx4 %[vo], %[srd], %[s1] offen" NTS " lds\n\t" \
    "s_add_u32 m0, m0, %[ms]\n\t" \
    "s_add_u32 %[s1], %[s1], %[d2]\n\t" \
    "buffer_load_dwordx4 %[vo], %[srd], %[s2] offen" NTS " lds\n\t" \
    "s_add_u32 m0, m0, %[ms]\n\t" \
    "s_nop 0\n\t" \
    "buffer_load_dwordx4 %[vo], %[srd], %[s1] offen" NTS " lds" \
    : [s1] "=&s"(s1), [s2] "=&s"(s2) \
    : [lds] "s"(lds), [so] "s"(so), [d1] "s"(d1), [d2] "s"(d2), [vo] "v"(vo), [srd] "s"(srd), [ms] "n"(MSTEP) \
    : "memory", "scc")
  if constexpr (NT) DMA4_BODY(" nt"); else DMA4_BODY("");
#undef DMA4_BODY
}
DI __amdgpu_buffer_rsrc_t mksrd(const void* p, unsigned bytes) { return __builtin_amdgcn_make_buffer_rsrc((void*)p, 0, bytes, 0x00020000); }

DI unsigned x_slot_rt(unsigned c) { return (c & 1u ? 2u : 4u) * SLOT; }
DI unsigned wq_slot_rt(unsigned i) { i = i >= 12u ? i - 12u : (i >= 6u ? i - 6u : i); return ((0x359876u >> (4u * i)) & 15u) * SLOT; }
DI unsigned wo_slot_rt(unsigned n) { n = n >= 10u ? n - 10u : n; return (unsigned)((0x7653429180ull >> (4u * n)) & 15ull) * SLOT; }

struct CW {
  f32x16 o[4];
  bf16x8 qf[8];
  f32x16 S[2][2];
  f32x16 negm;
  bf16x8 kf[16], vf[16];
  u32x4 pa[4];
  unsigned ka[8];
  float l, ps, mx, seed;
};

constexpr int LA = 8;
constexpr int GB = 32 - LA;
#define EXPD 5
constexpr int first_e(int g) {
  int n = 0;
  for (int i = 0; i < g; ++i) n += ((i & 3) == 1 && (i >> 2) < EXPD) ? 2 : 1;
  return n > 32 ? 32 : n;
}
constexpr int nlds(int g, bool QK, bool PV) { return g < 0 ? 1 : (g < 16 - LA ? (QK ? 1 : 0) : (g < GB ? (PV ? 2 : 0) : (QK ? 1 : 0))); }
template <int Y, int E> DI void sm_exp(CW& c) { c.S[Y][E >> 4][E & 15] = __builtin_amdgcn_exp2f(c.S[Y][E >> 4][E & 15]); }
template <int Y, int E> DI void sm_add(CW& c) { c.ps += c.S[Y][E >> 4][E & 15]; }
template <int Y, int C> DI void sm_cvt(CW& c) { c.pa[C >> 2][C & 3] = cvtpk(c.S[Y][C >> 3][(2 * C) & 15], c.S[Y][C >> 3][(2 * C + 1) & 15]); }
template <int X, int I> DI void sm_max(CW& c) { c.mx = mx3(c.mx, c.S[X][I >> 3][(2 * I) & 15], c.S[X][I >> 3][(2 * I + 1) & 15]); }
template <int X, bool QK, bool PV, int G> DI void att_gap(CW& c, const unsigned vcur) {
  constexpr int Y = X ^ 1;
  if constexpr (G == GB && QK) { BAR(); }
  if constexpr ((G & 3) == 0 && ((G < 16 && QK) || (G >= 16 && PV))) {
    constexpr int n = nlds(G - 4, QK, PV) + nlds(G - 3, QK, PV) + nlds(G - 2, QK, PV) + nlds(G - 1, QK, PV);
    LGKM(n);
    SB();
  }
  if constexpr (G < 16) {
    if constexpr (QK) {
      constexpr int kb = G >> 3, s = G & 7;
      if constexpr (s == 0) asm volatile("v_mfma_f32_32x32x16_bf16 %0, %1, %2, %3" : "=&v"(c.S[X][kb]) : "v"(c.kf[G]), "v"(c.qf[0]), "v"(c.negm));
      else c.S[X][kb] = mfma(c.kf[G], c.qf[s], c.S[X][kb]);
    }
  } else if constexpr (PV) {
    constexpr int j = G - 16, ks = j >> 2, db = j & 3;
    c.o[db] = mfma(c.vf[j], __builtin_bit_cast(bf16x8, c.pa[ks]), c.o[db]);
  }
  SB();
  if constexpr (G < 16 - LA && QK) { constexpr int n = G + LA; c.kf[n] = ldsb128(c.ka[n & 7] + (unsigned)X * SLOT + (n >> 3) * 8192u); }
  if constexpr (G >= GB && QK) { constexpr int n = G - GB; c.kf[n] = ldsb128(c.ka[n] + (unsigned)Y * SLOT); }
  if constexpr (G >= 16 - LA && G < GB && PV) { constexpr int j = G - (16 - LA), ks = j >> 2, db = j & 3; const unsigned a = vcur + db * 512u + ks * 4096u; c.vf[j] = ldtr2(a, a + 2048u); }
  SB();
  if constexpr (X == 1 && QK && G >= 10 && G < 24 && (G & 1) == 1 + 0 * G) { }
  if constexpr (PV) {
    constexpr int e0 = first_e(G), e1 = first_e(G + 1), p0 = G ? first_e(G - 1) : 0;
    if constexpr (G == 0) c.ps = 0.f;
    if constexpr (e0 < e1) sm_exp<Y, e0>(c);
    if constexpr (p0 < e0) sm_add<Y, p0>(c);
    if constexpr (p0 < e0 && (p0 & 1)) sm_cvt<Y, (p0 >> 1)>(c);
    if constexpr (e0 + 1 < e1) sm_exp<Y, e0 + 1>(c);
    if constexpr (p0 + 1 < e0) sm_add<Y, p0 + 1>(c);
    if constexpr (p0 + 1 < e0 && ((p0 + 1) & 1)) sm_cvt<Y, ((p0 + 1) >> 1)>(c);
    if constexpr (G == 28) { c.l += c.ps; }
    if constexpr (G <= 28) asm volatile("" : "+v"(c.ps), "+v"(c.l));
  }
  if constexpr (X == 1 && QK && G >= 8 && G < 24 && (G & 1) == 1) { constexpr int i = (G - 8) >> 1; c.ka[i] ^= 2u * SLOT; asm volatile("" : "+v"(c.ka[i])); }
  if constexpr (QK) {
    if constexpr (G == 27) { c.mx = c.seed; sm_max<X, 0>(c); }
    if constexpr (G == 28) { sm_max<X, 1>(c); sm_max<X, 2>(c); sm_max<X, 3>(c); }
    if constexpr (G == 29) { sm_max<X, 4>(c); sm_max<X, 5>(c); sm_max<X, 6>(c); sm_max<X, 7>(c); }
    if constexpr (G == 30) { sm_max<X, 8>(c); sm_max<X, 9>(c); sm_max<X, 10>(c); sm_max<X, 11>(c); }
    if constexpr (G == 31) {
      sm_max<X, 12>(c); sm_max<X, 13>(c); sm_max<X, 14>(c); sm_max<X, 15>(c);
    }
    if constexpr (G >= 27) asm volatile("" : "+v"(c.mx));
  }
  SB();
}
template <int X, bool QK, bool PV, int... Gs> DI void att_period_(CW& c, const unsigned vcur, const float thr, iseq<Gs...>) {
  (att_gap<X, QK, PV, Gs>(c, vcur), ...);
  asm volatile("" ::: "memory");
  if constexpr (QK) {
    {
      auto rr = __builtin_amdgcn_permlane32_swap(__float_as_uint(c.mx), __float_as_uint(c.mx), false, false);
      c.mx = mx3(__uint_as_float(rr[0]), __uint_as_float(rr[1]), c.mx);
    }
    if (__builtin_expect(__any(c.mx > thr), 0)) {
      const float dl = c.mx, f = __builtin_amdgcn_exp2f(-__builtin_fmaxf(dl, 0.f));
#pragma unroll
      for (int kb = 0; kb < 2; ++kb)
#pragma unroll
        for (int r = 0; r < 16; ++r) c.S[X][kb][r] -= dl;
#pragma unroll
      for (int db = 0; db < 4; ++db)
#pragma unroll
        for (int r = 0; r < 16; ++r) c.o[db][r] *= f;
      c.l *= f;
#pragma unroll
      for (int r = 0; r < 16; ++r) c.negm[r] -= dl;
      c.seed = 0.f;
      asm volatile("s_nop 1" : "+v"(c.negm));
    }
  }
  SB();
}
template <int X, bool QK, bool PV> DI void att_period(CW& c, const unsigned vcur, const float thr) { att_period_<X, QK, PV>(c, vcur, thr, mkseq<32>{}); }

struct QW { f32x16 acc[4]; bf16x8 a[2][8]; bf16x8 xf[2][2]; };
template <int I4, bool MM, int G> DI void q_gap(QW& q, const CW& c, const unsigned wa, const unsigned xoff, const unsigned r2) {
  constexpr int P = (I4 + 1) & 1, N = I4 & 1;
  if constexpr (MM) { constexpr int s = G >> 2, db = G & 3; q.acc[db] = mfma(q.a[P][G], q.xf[P][s], q.acc[db]); }
  SB();
  if constexpr (G == 0) q.xf[N][0] = ldsb128(((c.ka[0] ^ (64u * ((I4 + r2) & 3u))) + xoff));
  if constexpr (G == 3) q.xf[N][1] = ldsb128(((c.ka[0] ^ (64u * ((I4 + r2) & 3u) + 32u)) + xoff));
  if constexpr (G < 6) {
    constexpr int f0 = (8 * G) / 6, f1 = (8 * (G + 1)) / 6;
    if constexpr (f0 < f1) { constexpr int f = f0; const unsigned a = wa + (f >> 2) * 8192u + (f & 3) * 512u; q.a[N][f] = ldtr2(a, a + 256u); }
    if constexpr (f0 + 1 < f1) { constexpr int f = f0 + 1; const unsigned a = wa + (f >> 2) * 8192u + (f & 3) * 512u; q.a[N][f] = ldtr2(a, a + 256u); }
  }
  SB();
}
template <int I4, bool MM, int... Gs> DI void q_step_(QW& q, const CW& c, const unsigned wa, const unsigned xoff, const unsigned r2, iseq<Gs...>) { (q_gap<I4, MM, Gs>(q, c, wa, xoff, r2), ...); }
template <int I4, bool MM> DI void q_step(QW& q, const CW& c, const unsigned wa, const unsigned xoff, const unsigned r2) { if constexpr (I4 == 0 && MM) LGKM(0); BAR(); SB(); q_step_<I4, MM>(q, c, wa, xoff, r2, mkseq<8>{}); asm volatile("" ::: "memory"); }

struct OW { bf16x8 of[16]; bf16x8 a[2][16]; f32x16 acc[2]; };
DI void o_store(const f32x16& acc, __amdgpu_buffer_rsrc_t srd, unsigned voff, unsigned soff, int u) {
  const u32x4 v = {cvtpk(acc[8 * u], acc[8 * u + 1]), cvtpk(acc[8 * u + 2], acc[8 * u + 3]), cvtpk(acc[8 * u + 4], acc[8 * u + 5]), cvtpk(acc[8 * u + 6], acc[8 * u + 7])};
  __builtin_amdgcn_raw_buffer_store_b128(v, srd, voff + 1024u * u, soff, 16  );
}
template <int N, bool MM, int G> DI void o_gap(OW& w, const unsigned a0, const unsigned a1, __amdgpu_buffer_rsrc_t srd, const unsigned voff, const unsigned soff) {
  constexpr int P = N ^ 1;
  if constexpr (MM) { if constexpr (G == 0) w.acc[P] = mfma(w.a[P][0], w.of[0], f32x16{}); else w.acc[P] = mfma(w.a[P][G], w.of[G], w.acc[P]); }
  SB();
  if constexpr (G < 14) {
    constexpr int f0 = (16 * G) / 14, f1 = (16 * (G + 1)) / 14;
    if constexpr (f0 < f1) { constexpr int f = f0; const unsigned a = ((f >> 3) ? a1 : a0) + (f & 7) * 2048u; w.a[N][f] = ldtr2(a, a + 1024u); }
    if constexpr (f0 + 1 < f1) { constexpr int f = f0 + 1; const unsigned a = ((f >> 3) ? a1 : a0) + (f & 7) * 2048u; w.a[N][f] = ldtr2(a, a + 1024u); }
  }
  if constexpr (MM && (G == 4 || G == 6)) o_store(w.acc[N], srd, voff, soff, (G - 4) >> 1);
  SB();
}
template <int N, bool MM, int... Gs> DI void o_step_(OW& w, const unsigned a0, const unsigned a1, __amdgpu_buffer_rsrc_t srd, const unsigned voff, const unsigned soff, iseq<Gs...>) {
  (o_gap<N, MM, Gs>(w, a0, a1, srd, voff, soff), ...);
  asm volatile("" ::: "memory");
}

__global__ __launch_bounds__(512, 2) void fused_kernel(const char* __restrict__ x, const char* __restrict__ kc, const char* __restrict__ vc,
                                                       const char* __restrict__ wq, const char* __restrict__ wo, char* __restrict__ out, char* __restrict__ ws) {
  unsigned long long pc0;
  asm volatile("s_getpc_b64 %0" : "=s"(pc0));
  extern __shared__ __attribute__((aligned(16))) char lds[];
  const unsigned ldsb = (unsigned)(uintptr_t)lds;
  const unsigned lane = threadIdx.x & 63u;
  const unsigned wid = __builtin_amdgcn_readfirstlane(threadIdx.x >> 6);
  const unsigned kvh = blockIdx.x & 7u, half = (blockIdx.x >> 3) & 1u, b = blockIdx.x >> 4;
  const unsigned jw = blockIdx.x & 15u;

  if (wid >= 4) {
#define CODE_KB 23
    if (wid == 7 && (blockIdx.x >> 3) < CODE_KB)
      __builtin_amdgcn_global_load_lds((const GAS void*)(uintptr_t)((pc0 & ~255ull) + (blockIdx.x >> 3) * 1024u + lane * 16u), (LAS void*)(uintptr_t)(ldsb + SLOT), 16, 0, 0);
    const unsigned hw = wid - 4u;
    const unsigned r4 = lane >> 4, c16 = lane & 15u;
    const unsigned swz = 16u * (c16 ^ (4u * hw + r4));
    const unsigned r8 = (lane >> 2) & 7u, cb = 64u * (lane >> 5) + 16u * (lane & 3u);
    const unsigned ld = ldsb + hw * 1024u;
    const unsigned rq = (b + 2u * half) & 3u, r2 = (b >> 2) & 3u, ro = b & 7u;
    const __amdgpu_buffer_rsrc_t sx = mksrd(x + (size_t)b * SQ * DM * 2, SQ * DM * 2);
    const __amdgpu_buffer_rsrc_t swq = mksrd(wq + (size_t)(kvh * 512 + half * 256) * 2, DM * HQ * DH * 2 - (kvh * 512 + half * 256) * 2);
    const unsigned vo_x = (4u * hw + r4) * 1024u + swz;
    const unsigned vo_wq = r8 * 8192u + 128u * hw + cb;
#define DMA_X(c)  dma4<W_NT, 4096>(sx, vo_x, (((c) + rq) & 3u) * 256u, 16u * 1024u, 32u * 1024u, ld + x_slot_rt(c))
#define DMA_WQ(i) dma4<W_NT, 4096>(swq, vo_wq, (4u * ((((i) >> 2) + rq) & 3u) + (((i) + r2) & 3u)) * 32u * 8192u, 8u * 8192u, 16u * 8192u, ld + wq_slot_rt(i))
    TSH(26);
    DMA_X(0u);
#pragma nounroll
    for (unsigned i = 0; i < 3; ++i) DMA_WQ(i);
    TSH(28);
    const __amdgpu_buffer_rsrc_t sk = mksrd(kc + (size_t)(b * HKV + kvh) * SK * DH * 2, SK * DH * 2);
    const __amdgpu_buffer_rsrc_t sv = mksrd(vc + (size_t)(b * HKV + kvh) * SK * DH * 2, SK * DH * 2);
    const __amdgpu_buffer_rsrc_t swo = mksrd(wo + (size_t)(kvh * 512 + half * 256) * 1024, 256 * 1024);
    const unsigned vo_k = (4u * hw + r4) * 256u + swz;
    const unsigned vo_v = ((hw >> 1) * 8u + r8) * 256u + 128u * (hw & 1u) + cb;
    const unsigned vo_wo = (hw * 8u + r8) * 1024u + cb;
#define DMA_K(t)  dma4<KV_NT, 4096>(sk, vo_k, (t) * 16384u, 4096u, 8192u, ld + ((t) & 3u) * SLOT)
#define DMA_V(t, vs)  dma4<KV_NT, 4096>(sv, vo_v, (t) * 16384u, 4096u, 8192u, ld + (4u + (vs)) * SLOT)
#define DMA_WO(n) dma4<W_NT, 4096>(swo, vo_wo, ((n) & 1u) * 128u * 1024u + ((((n) >> 1) + ro) & 7u) * 128u, 32u * 1024u, 64u * 1024u, ld + wo_slot_rt(n))
    VMW(8);  BAR(); TSH(30); DMA_WQ(3u); DMA_X(1u);
    VMW(12); BAR(); DMA_WQ(4u);
    VMW(12); BAR(); DMA_WQ(5u);
    VMW(12); BAR(); DMA_WQ(6u);
    VMW(8);  BAR(); DMA_WQ(7u);
    VMW(8);  BAR(); DMA_WQ(8u); DMA_X(2u);
    VMW(12); BAR(); DMA_WQ(9u);
    VMW(12); BAR(); DMA_WQ(10u);
    VMW(8);  BAR(); DMA_WQ(11u);
    VMW(8);  BAR(); DMA_WQ(12u); DMA_X(3u);
    VMW(12); BAR(); DMA_WQ(13u);
    VMW(12); BAR(); DMA_WQ(14u); DMA_K(0u);
    VMW(12); BAR(); DMA_WQ(15u);
    VMW(12); BAR(); DMA_K(1u);
    VMW(12); BAR(); DMA_V(0u, 0u);
    VMW(8);  BAR(); DMA_V(1u, 1u);
    VMW(12);
    {
      BAR();
      DMA_K(2u); DMA_K(3u); DMA_V(2u, 2u);
      VMW(16);
      BAR();
      DMA_K(4u); DMA_V(3u, 3u);
      VMW(16);
      unsigned vi = 4;
#pragma nounroll
      for (unsigned u = 2; u <= 12; ++u) {
        BAR();
        DMA_K(u + 3u); DMA_V(u + 2u, vi);
        VMW(16);
        vi = vi == 5u ? 0u : vi + 1u;
      }
      BAR(); DMA_V(15u, vi); DMA_WO(0u); DMA_WO(1u);
      VMW(20);
      BAR(); DMA_WO(2u); DMA_WO(3u);
      VMW(20);
      BAR(); DMA_WO(4u); DMA_WO(5u);
      VMW(24);
    }
    BAR(); DMA_WO(6u); DMA_WO(7u);
    BAR();
    VMW(24); BAR();
    VMW(16); BAR();
    VMW(8);  BAR();
#pragma nounroll
    for (unsigned n = 8; n < 12; ++n) DMA_WO(n);
    VMW(16); BAR();
    DMA_WO(12u); DMA_WO(13u);
    VMW(16); BAR();
    DMA_WO(14u); DMA_WO(15u);
    VMW(16); BAR();
    VMW(8);  BAR();
    VMW(0);  BAR();
    GAS unsigned* ep = (GAS unsigned*)(uintptr_t)(ws + WS_EPOCH + b * 128u);
    unsigned tag = 0;
    if (wid == 4) tag = __hip_atomic_load(ep, __ATOMIC_RELAXED, __HIP_MEMORY_SCOPE_AGENT) + 1u;
    BAR();
    BAR();
    if (wid == 4) {
      GAS unsigned* fl = (GAS unsigned*)(uintptr_t)(ws + WS_FLAG + (b * 16u) * 128u);
      tag = __builtin_amdgcn_readfirstlane(tag);
      if (lane == 0) __hip_atomic_store(fl + jw * 32u, tag, __ATOMIC_RELAXED, __HIP_MEMORY_SCOPE_AGENT);
      for (unsigned spins = 0; spins < (1u << 22); ++spins) {
        const unsigned v = __hip_atomic_load(fl + (lane & 15u) * 32u, __ATOMIC_RELAXED, __HIP_MEMORY_SCOPE_AGENT);
        if (__all(v == tag)) break;
        __builtin_amdgcn_s_sleep(1);
      }
      if (lane == 0 && jw == 0) __hip_atomic_store(ep, tag, __ATOMIC_RELAXED, __HIP_MEMORY_SCOPE_AGENT);
      __builtin_amdgcn_fence(__ATOMIC_ACQUIRE, "wavefront");
    }
    BAR();
    return;
  }

  TS(0);
  const unsigned hh = wid >> 1, qh = wid & 1u, l31 = lane & 31u, h = lane >> 5;
  CW c;
#pragma unroll
  for (int s = 0; s < 8; ++s) c.ka[s] = ldsb + l31 * 256u + 16u * ((2u * s + h) ^ (lane & 15u));
  const unsigned trq = (lane >> 2) & 3u, trp = lane & 3u, trb = (lane >> 4) & 1u;
  const unsigned tr_nat = ldsb + 64u * trq + 32u * trb + 8u * (((trp & 1u) << 1) | (trp >> 1));
  const unsigned tr_v = ldsb + 64u * trq + 32u * trb + 8u * trp + 256u * h;

  {
    QW q;
#pragma unroll
    for (int db = 0; db < 4; ++db) q.acc[db] = f32x16{};
    const unsigned wa = tr_nat + h * 4096u + hh * 2048u;
    const unsigned r2 = (b >> 2) & 3u;
    auto wbase = [&](unsigned i) { unsigned a = wa + wq_slot_rt(i); asm volatile("" : "+v"(a)); return a; };
    q_step<0, false>(q, c, wbase(0u), x_slot_rt(0) + qh * 8192u, r2);
    TS(1);
#pragma nounroll
    for (unsigned cc = 0; cc < 4; ++cc) {
      const unsigned xo = x_slot_rt(cc) + qh * 8192u, xn = x_slot_rt((cc + 1u) & 3u) + qh * 8192u;
      q_step<1, true>(q, c, wbase(4u * cc + 1u), xo, r2);
      q_step<2, true>(q, c, wbase(4u * cc + 2u), xo, r2);
      q_step<3, true>(q, c, wbase(4u * cc + 3u), xo, r2);
      q_step<0, true>(q, c, wbase((4u * cc + 4u) & 15u), xn, r2);
    }
    TS(4);
#pragma unroll
    for (int n = 0; n < LA; ++n) c.kf[n] = ldsb128(c.ka[n]);
#pragma unroll
    for (int db = 0; db < 4; ++db)
#pragma unroll
      for (int u = 0; u < 2; ++u) {
        u32x4 w;
#pragma unroll
        for (int e = 0; e < 4; ++e) w[e] = cvtpk(q.acc[db][8 * u + 2 * e] * QSCALE, q.acc[db][8 * u + 2 * e + 1] * QSCALE);
        c.qf[2 * db + u] = __builtin_bit_cast(bf16x8, w);
      }
  }
  TS(5);
#pragma unroll
  for (int db = 0; db < 4; ++db) c.o[db] = f32x16{};
#pragma unroll
  for (int r = 0; r < 16; ++r) { c.S[1][0][r] = -3.0e38f; c.S[1][1][r] = -3.0e38f; c.negm[r] = 0.f; }
  c.l = 0.f; c.ps = 0.f; c.mx = 0.f; c.seed = -3.0e38f;
  asm volatile("s_nop 1" : "+v"(c.negm), "+v"(c.qf[0]));
  unsigned vslot = 5;
  auto vnext = [&]() { const unsigned a = tr_v + (4u + vslot) * SLOT; vslot = vslot == 5 ? 0 : vslot + 1; return a; };
  SB();
#pragma nounroll
  for (int t = 0; t < 16; t += 2) {
    att_period<0, true, true>(c, vnext(), t == 0 ? -__builtin_inff() : THR);
    att_period<1, true, true>(c, vnext(), THR);
  }
  TS(10);
  att_period<0, false, true>(c, vnext(), THR);
  TS(11);

  {
    auto rr = __builtin_amdgcn_permlane32_swap(__float_as_uint(c.l), __float_as_uint(c.l), false, false);
    c.l = __uint_as_float(rr[0]) + __uint_as_float(rr[1]);
  }
  const float il = 1.0f / c.l;
  OW w;
  BARL();
  {
    const unsigned xw = ldsb + 6u * SLOT + wid * 8192u + lane * 16u;
    const unsigned xr = ldsb + 6u * SLOT + (wid ^ 2u) * 8192u + lane * 16u;
    bf16x8 own[8];
#pragma unroll
    for (int db = 0; db < 4; ++db)
#pragma unroll
      for (int u = 0; u < 2; ++u) {
        u32x4 v;
#pragma unroll
        for (int e = 0; e < 4; ++e) v[e] = cvtpk(c.o[db][8 * u + 2 * e] * il, c.o[db][8 * u + 2 * e + 1] * il);
        own[2 * db + u] = __builtin_bit_cast(bf16x8, v);
        *(LAS bf16x8*)(uintptr_t)(xw + (2 * db + u) * 1024u) = own[2 * db + u];
      }
    BARL();
    TS(12);
#pragma unroll
    for (int s = 0; s < 8; ++s) {
      const bf16x8 oth = ldsb128(xr + s * 1024u);
      w.of[s] = hh == 0 ? own[s] : oth;
      w.of[8 + s] = hh == 0 ? oth : own[s];
    }
  }
  const __amdgpu_buffer_rsrc_t ssl = mksrd(ws + WS_SLAB, 256u * SLAB_WG);
  {
    const unsigned wa = tr_v + hh * 512u;
    const unsigned ro = b & 7u;
    const unsigned voff = lane * 16u, sbase = blockIdx.x * SLAB_WG + wid * FRAG_B;
    w.acc[0] = f32x16{}; w.acc[1] = f32x16{};
    o_step_<0, false>(w, wa + wo_slot_rt(0), wa + wo_slot_rt(1), ssl, voff, 0u, mkseq<16>{});
#pragma nounroll
    for (unsigned C = 1; C < 9; C += 2) {
      BAR(); SB();
      o_step_<1, true>(w, wa + wo_slot_rt(2 * C), wa + wo_slot_rt(2 * C + 1), ssl, voff, sbase + (C == 1 ? 32u : 4u * ((C - 2u + ro) & 7u)) * FRAG_B, mkseq<16>{});
      BAR(); SB();
      o_step_<0, true>(w, wa + wo_slot_rt(C == 7 ? 0u : 2 * C + 2), wa + wo_slot_rt(C == 7 ? 1u : 2 * C + 3), ssl, voff, sbase + 4u * ((C - 1u + ro) & 7u) * FRAG_B, mkseq<16>{});
    }
    TS(14);
    o_store(w.acc[1], ssl, voff, sbase + 4u * ((7u + ro) & 7u) * FRAG_B, 0);
    o_store(w.acc[1], ssl, voff, sbase + 4u * ((7u + ro) & 7u) * FRAG_B, 1);
  }
  TS(24);
  VMW(0);
  TS(25);
  BAR();
  BAR();
  TS(16);
  {
    const unsigned f = wid >> 1, u = wid & 1u;
    const unsigned frag = (jw >> 1) * 4u + 2u * (jw & 1u) + f;
    const unsigned voff = u * 1024u + lane * 16u, soff = b * 16u * SLAB_WG + frag * FRAG_B;
    u32x4 v[16];
#pragma unroll
    for (int p = 0; p < 16; ++p) v[p] = __builtin_amdgcn_raw_buffer_load_b128(ssl, voff, soff + p * SLAB_WG, 16  );
    float s[8] = {0.f, 0.f, 0.f, 0.f, 0.f, 0.f, 0.f, 0.f};
#pragma unroll
    for (int p = 0; p < 16; ++p)
#pragma unroll
      for (int e = 0; e < 4; ++e) { s[2 * e] += __uint_as_float(v[p][e] << 16); s[2 * e + 1] += __uint_as_float(v[p][e] & 0xffff0000u); }
    unsigned a0 = cvtpk(s[0], s[1]), a1 = cvtpk(s[2], s[3]), b0 = cvtpk(s[4], s[5]), b1 = cvtpk(s[6], s[7]);
    auto r0 = __builtin_amdgcn_permlane32_swap(a0, b0, false, false);
    auto r1 = __builtin_amdgcn_permlane32_swap(a1, b1, false, false);
    const u32x4 o4 = {r0[0], r1[0], r0[1], r1[1]};
    const unsigned q = 32u * f + l31, n = 32u * jw + 16u * u + 8u * h;
    *(GAS u32x4*)(uintptr_t)(out + ((size_t)(b * SQ + q) * DM + n) * 2) = o4;
  }
}

extern "C" void kernel_launch(void* const* d_in, const int* in_sizes, int n_in, void* d_out, int out_size, void* d_ws, size_t ws_size, hipStream_t stream) {
  static int ok = 0;
  if (!ok) { (void)hipFuncSetAttribute((const void*)fused_kernel, hipFuncAttributeMaxDynamicSharedMemorySize, (int)LDS_BYTES); ok = 1; }
  hipLaunchKernelGGL(fused_kernel, dim3(256), dim3(512), LDS_BYTES, stream, (const char*)d_in[0], (const char*)d_in[1], (const char*)d_in[2],
                     (const char*)d_in[3], (const char*)d_in[4], (char*)d_out, (char*)d_ws);
}
```

```cpp
#include <hip/hip_runtime.h>
#include <stdint.h>
#include <utility>

typedef __bf16 bf16x8 __attribute__((ext_vector_type(8)));
typedef __bf16 bf16x4 __attribute__((ext_vector_type(4)));
typedef __bf16 bf16x2 __attribute__((ext_vector_type(2)));
typedef float f32x16 __attribute__((ext_vector_type(16)));
typedef float f32x4 __attribute__((ext_vector_type(4)));
typedef float f32x2 __attribute__((ext_vector_type(2)));
typedef unsigned u32x4 __attribute__((ext_vector_type(4)));
typedef unsigned u32x2 __attribute__((ext_vector_type(2)));
#define LAS __attribute__((address_space(3)))
#define GAS __attribute__((address_space(1)))
#define DI __device__ __forceinline__
template <int... Is> using iseq = std::integer_sequence<int, Is...>;
template <int N> using mkseq = std::make_integer_sequence<int, N>;

constexpr int NB = 16, SQ = 64, DM = 512, HQ = 32, HKV = 8, SK = 1024, DH = 128;
constexpr unsigned SLOT = 16384u;
constexpr unsigned LDS_BYTES = 10 * SLOT;
constexpr float QSCALE = 0.12751743082f;
constexpr float THR = 8.0f;
constexpr unsigned WS_EPOCH = 0;
constexpr unsigned WS_FLAG = 4096;
constexpr unsigned WS_SLAB = 65536;
constexpr unsigned FRAG_B = 2048;
constexpr unsigned SLAB_WG = 36 * FRAG_B;
#define KV_NT true
#define W_NT false

#define BAR() asm volatile("s_barrier" ::: "memory")
#define LGKM(n) __builtin_amdgcn_s_waitcnt(0xC07F | ((n) << 8))
#define BARL() do { LGKM(0); BAR(); } while (0)
#define VMW(n) asm volatile("s_waitcnt vmcnt(%0)" :: "n"(n) : "memory")
#define SB() __builtin_amdgcn_sched_barrier(0)
#define TS(i) do {} while (0)
#define TSH(i) do {} while (0)

DI unsigned cvtpk(float lo, float hi) { f32x2 v = {lo, hi}; bf16x2 b = __builtin_convertvector(v, bf16x2); return __builtin_bit_cast(unsigned, b); }
DI bf16x8 ldsb128(unsigned a) { return *(const LAS bf16x8*)(uintptr_t)a; }
DI bf16x8 ldtr2(unsigned a0, unsigned a1) {
  bf16x4 lo = __builtin_amdgcn_ds_read_tr16_b64_v4bf16((LAS bf16x4*)(uintptr_t)a0);
  bf16x4 hi = __builtin_amdgcn_ds_read_tr16_b64_v4bf16((LAS bf16x4*)(uintptr_t)a1);
  return __builtin_shufflevector(lo, hi, 0, 1, 2, 3, 4, 5, 6, 7);
}
DI f32x16 mfma(bf16x8 a, bf16x8 b, f32x16 c) { return __builtin_amdgcn_mfma_f32_32x32x16_bf16(a, b, c, 0, 0, 0); }
DI float mx3(float a, float b, float c) { return __builtin_fmaxf(__builtin_fmaxf(a, b), c); }

template <bool NT, int MSTEP> DI void dma4(__amdgpu_buffer_rsrc_t srd, unsigned vo, unsigned so, unsigned d1, unsigned d2, unsigned lds) {
  unsigned s1, s2;
#define DMA4_BODY(NTS) \
  asm volatile( \
    "s_mov_b32 m0, %[lds]\n\t" \
    "s_add_u32 %[s1], %[so], %[d1]\n\t" \
    "buffer_load_dwordx4 %[vo], %[srd], %[so] offen" NTS " lds\n\t" \
    "s_add_u32 m0, m0, %[ms]\n\t" \
    "s_add_u32 %[s2], %[so], %[d2]\n\t" \
    "buffer_load_dwordx4 %[vo], %[srd], %[s1] offen" NTS " lds\n\t" \
    "s_add_u32 m0, m0, %[ms]\n\t" \
    "s_add_u32 %[s1], %[s1], %[d2]\n\t" \
    "buffer_load_dwordx4 %[vo], %[srd], %[s2] offen" NTS " lds\n\t" \
    "s_add_u32 m0, m0, %[ms]\n\t" \
    "s_nop 0\n\t" \
    "buffer_load_dwordx4 %[vo], %[srd], %[s1] offen" NTS " lds" \
    : [s1] "=&s"(s1), [s2] "=&s"(s2) \
    : [lds] "s"(lds), [so] "s"(so), [d1] "s"(d1), [d2] "s"(d2), [vo] "v"(vo), [srd] "s"(srd), [ms] "n"(MSTEP) \
    : "memory", "scc")
  if constexpr (NT) DMA4_BODY(" nt"); else DMA4_BODY("");
#undef DMA4_BODY
}
DI __amdgpu_buffer_rsrc_t mksrd(const void* p, unsigned bytes) { return __builtin_amdgcn_make_buffer_rsrc((void*)p, 0, bytes, 0x00020000); }

DI unsigned x_slot_rt(unsigned c) { return (c & 1u ? 2u : 4u) * SLOT; }
DI unsigned wq_slot_rt(unsigned i) { i = i >= 12u ? i - 12u : (i >= 6u ? i - 6u : i); return ((0x359876u >> (4u * i)) & 15u) * SLOT; }
DI unsigned wo_slot_rt(unsigned n) { n = n >= 10u ? n - 10u : n; return (unsigned)((0x7653429180ull >> (4u * n)) & 15ull) * SLOT; }

struct CW {
  f32x16 o[4];
  bf16x8 qf[8];
  f32x16 S[2][2];
  f32x16 negm;
  bf16x8 kf[16], vf[16];
  u32x4 pa[4];
  unsigned ka[8];
  float l, ps, mx, seed;
};

constexpr int LA = 8;
constexpr int GB = 32 - LA;
#define EXPD 5
constexpr int first_e(int g) {
  int n = 0;
  for (int i = 0; i < g; ++i) n += ((i & 3) == 1 && (i >> 2) < EXPD) ? 2 : 1;
  return n > 32 ? 32 : n;
}
constexpr int nlds(int g, bool QK, bool PV) { return g < 0 ? 1 : (g < 16 - LA ? (QK ? 1 : 0) : (g < GB ? (PV ? 2 : 0) : (QK ? 1 : 0))); }
template <int Y, int E> DI void sm_exp(CW& c) { c.S[Y][E >> 4][E & 15] = __builtin_amdgcn_exp2f(c.S[Y][E >> 4][E & 15]); }
template <int Y, int E> DI void sm_add(CW& c) { c.ps += c.S[Y][E >> 4][E & 15]; }
template <int Y, int C> DI void sm_cvt(CW& c) { c.pa[C >> 2][C & 3] = cvtpk(c.S[Y][C >> 3][(2 * C) & 15], c.S[Y][C >> 3][(2 * C + 1) & 15]); }
template <int X, int I> DI void sm_max(CW& c) { c.mx = mx3(c.mx, c.S[X][I >> 3][(2 * I) & 15], c.S[X][I >> 3][(2 * I + 1) & 15]); }
template <int X, bool QK, bool PV, int G> DI void att_gap(CW& c, const unsigned vcur) {
  constexpr int Y = X ^ 1;
  if constexpr (G == GB && QK) { BAR(); }
  if constexpr ((G & 3) == 0 && ((G < 16 && QK) || (G >= 16 && PV))) {
    constexpr int n = nlds(G - 4, QK, PV) + nlds(G - 3, QK, PV) + nlds(G - 2, QK, PV) + nlds(G - 1, QK, PV);
    LGKM(n);
    SB();
  }
  if constexpr (G < 16) {
    if constexpr (QK) {
      constexpr int kb = G >> 3, s = G & 7;
      if constexpr (s == 0) asm volatile("v_mfma_f32_32x32x16_bf16 %0, %1, %2, %3" : "=&v"(c.S[X][kb]) : "v"(c.kf[G]), "v"(c.qf[0]), "v"(c.negm));
      else c.S[X][kb] = mfma(c.kf[G], c.qf[s], c.S[X][kb]);
    }
  } else if constexpr (PV) {
    constexpr int j = G - 16, ks = j >> 2, db = j & 3;
    c.o[db] = mfma(c.vf[j], __builtin_bit_cast(bf16x8, c.pa[ks]), c.o[db]);
  }
  SB();
  if constexpr (G < 16 - LA && QK) { constexpr int n = G + LA; c.kf[n] = ldsb128(c.ka[n & 7] + (unsigned)X * SLOT + (n >> 3) * 8192u); }
  if constexpr (G >= GB && QK) { constexpr int n = G - GB; c.kf[n] = ldsb128(c.ka[n] + (unsigned)Y * SLOT); }
  if constexpr (G >= 16 - LA && G < GB && PV) { constexpr int j = G - (16 - LA), ks = j >> 2, db = j & 3; const unsigned a = vcur + db * 512u + ks * 4096u; c.vf[j] = ldtr2(a, a + 2048u); }
  SB();
  if constexpr (X == 1 && QK && G >= 10 && G < 24 && (G & 1) == 1 + 0 * G) { }
  if constexpr (PV) {
    constexpr int e0 = first_e(G), e1 = first_e(G + 1), p0 = G ? first_e(G - 1) : 0;
    if constexpr (G == 0) c.ps = 0.f;
    if constexpr (e0 < e1) sm_exp<Y, e0>(c);
    if constexpr (p0 < e0) sm_add<Y, p0>(c);
    if constexpr (p0 < e0 && (p0 & 1)) sm_cvt<Y, (p0 >> 1)>(c);
    if constexpr (e0 + 1 < e1) sm_exp<Y, e0 + 1>(c);
    if constexpr (p0 + 1 < e0) sm_add<Y, p0 + 1>(c);
    if constexpr (p0 + 1 < e0 && ((p0 + 1) & 1)) sm_cvt<Y, ((p0 + 1) >> 1)>(c);
    if constexpr (G == 28) { c.l += c.ps; }
    if constexpr (G <= 28) asm volatile("" : "+v"(c.ps), "+v"(c.l));
  }
  if constexpr (X == 1 && QK && G >= 8 && G < 24 && (G & 1) == 1) { constexpr int i = (G - 8) >> 1; c.ka[i] ^= 2u * SLOT; asm volatile("" : "+v"(c.ka[i])); }
  if constexpr (QK) {
    if constexpr (G == 27) { c.mx = c.seed; sm_max<X, 0>(c); }
    if constexpr (G == 28) { sm_max<X, 1>(c); sm_max<X, 2>(c); sm_max<X, 3>(c); }
    if constexpr (G == 29) { sm_max<X, 4>(c); sm_max<X, 5>(c); sm_max<X, 6>(c); sm_max<X, 7>(c); }
    if constexpr (G == 30) { sm_max<X, 8>(c); sm_max<X, 9>(c); sm_max<X, 10>(c); sm_max<X, 11>(c); }
    if constexpr (G == 31) {
      sm_max<X, 12>(c); sm_max<X, 13>(c); sm_max<X, 14>(c); sm_max<X, 15>(c);
    }
    if constexpr (G >= 27) asm volatile("" : "+v"(c.mx));
  }
  SB();
}
template <int X, bool QK, bool PV, int... Gs> DI void att_period_(CW& c, const unsigned vcur, const float thr, iseq<Gs...>) {
  (att_gap<X, QK, PV, Gs>(c, vcur), ...);
  asm volatile("" ::: "memory");
  if constexpr (QK) {
    {
      auto rr = __builtin_amdgcn_permlane32_swap(__float_as_uint(c.mx), __float_as_uint(c.mx), false, false);
      c.mx = mx3(__uint_as_float(rr[0]), __uint_as_float(rr[1]), c.mx);
    }
    if (__builtin_expect(__any(c.mx > thr), 0)) {
      const float dl = c.mx, f = __builtin_amdgcn_exp2f(-__builtin_fmaxf(dl, 0.f));
#pragma unroll
      for (int kb = 0; kb < 2; ++kb)
#pragma unroll
        for (int r = 0; r < 16; ++r) c.S[X][kb][r] -= dl;
#pragma unroll
      for (int db = 0; db < 4; ++db)
#pragma unroll
        for (int r = 0; r < 16; ++r) c.o[db][r] *= f;
      c.l *= f;
#pragma unroll
      for (int r = 0; r < 16; ++r) c.negm[r] -= dl;
      c.seed = 0.f;
      asm volatile("s_nop 1" : "+v"(c.negm));
    }
  }
  SB();
}
template <int X, bool QK, bool PV> DI void att_period(CW& c, const unsigned vcur, const float thr) { att_period_<X, QK, PV>(c, vcur, thr, mkseq<32>{}); }

struct QW { f32x16 acc[4]; bf16x8 a[2][8]; bf16x8 xf[2][2]; };
template <int I4, bool MM, int G> DI void q_gap(QW& q, const CW& c, const unsigned wa, const unsigned xoff, const unsigned r2) {
  constexpr int P = (I4 + 1) & 1, N = I4 & 1;
  if constexpr (MM) { constexpr int s = G >> 2, db = G & 3; q.acc[db] = mfma(q.a[P][G], q.xf[P][s], q.acc[db]); }
  SB();
  if constexpr (G == 0) q.xf[N][0] = ldsb128(((c.ka[0] ^ (64u * ((I4 + r2) & 3u))) + xoff));
  if constexpr (G == 3) q.xf[N][1] = ldsb128(((c.ka[0] ^ (64u * ((I4 + r2) & 3u) + 32u)) + xoff));
  if constexpr (G < 6) {
    constexpr int f0 = (8 * G) / 6, f1 = (8 * (G + 1)) / 6;
    if constexpr (f0 < f1) { constexpr int f = f0; const unsigned a = wa + (f >> 2) * 8192u + (f & 3) * 512u; q.a[N][f] = ldtr2(a, a + 256u); }
    if constexpr (f0 + 1 < f1) { constexpr int f = f0 + 1; const unsigned a = wa + (f >> 2) * 8192u + (f & 3) * 512u; q.a[N][f] = ldtr2(a, a + 256u); }
  }
  SB();
}
template <int I4, bool MM, int... Gs> DI void q_step_(QW& q, const CW& c, const unsigned wa, const unsigned xoff, const unsigned r2, iseq<Gs...>) { (q_gap<I4, MM, Gs>(q, c, wa, xoff, r2), ...); }
template <int I4, bool MM> DI void q_step(QW& q, const CW& c, const unsigned wa, const unsigned xoff, const unsigned r2) { if constexpr (I4 == 0 && MM) LGKM(0); BAR(); SB(); q_step_<I4, MM>(q, c, wa, xoff, r2, mkseq<8>{}); asm volatile("" ::: "memory"); }

struct OW { bf16x8 of[16]; bf16x8 a[2][16]; f32x16 acc[2]; };
DI void o_store(const f32x16& acc, __amdgpu_buffer_rsrc_t srd, unsigned voff, unsigned soff, int u) {
  const u32x4 v = {cvtpk(acc[8 * u], acc[8 * u + 1]), cvtpk(acc[8 * u + 2], acc[8 * u + 3]), cvtpk(acc[8 * u + 4], acc[8 * u + 5]), cvtpk(acc[8 * u + 6], acc[8 * u + 7])};
  __builtin_amdgcn_raw_buffer_store_b128(v, srd, voff + 1024u * u, soff, 16  );
}
template <int N, bool MM, int G> DI void o_gap(OW& w, const unsigned a0, const unsigned a1, __amdgpu_buffer_rsrc_t srd, const unsigned voff, const unsigned soff) {
  constexpr int P = N ^ 1;
  if constexpr (MM) { if constexpr (G == 0) w.acc[P] = mfma(w.a[P][0], w.of[0], f32x16{}); else w.acc[P] = mfma(w.a[P][G], w.of[G], w.acc[P]); }
  SB();
  if constexpr (G < 14) {
    constexpr int f0 = (16 * G) / 14, f1 = (16 * (G + 1)) / 14;
    if constexpr (f0 < f1) { constexpr int f = f0; const unsigned a = ((f >> 3) ? a1 : a0) + (f & 7) * 2048u; w.a[N][f] = ldtr2(a, a + 1024u); }
    if constexpr (f0 + 1 < f1) { constexpr int f = f0 + 1; const unsigned a = ((f >> 3) ? a1 : a0) + (f & 7) * 2048u; w.a[N][f] = ldtr2(a, a + 1024u); }
  }
  if constexpr (MM && (G == 4 || G == 6)) o_store(w.acc[N], srd, voff, soff, (G - 4) >> 1);
  SB();
}
template <int N, bool MM, int... Gs> DI void o_step_(OW& w, const unsigned a0, const unsigned a1, __amdgpu_buffer_rsrc_t srd, const unsigned voff, const unsigned soff, iseq<Gs...>) {
  (o_gap<N, MM, Gs>(w, a0, a1, srd, voff, soff), ...);
  asm volatile("" ::: "memory");
}

__global__ __launch_bounds__(512, 2) void fused_kernel(const char* __restrict__ x, const char* __restrict__ kc, const char* __restrict__ vc,
                                                       const char* __restrict__ wq, const char* __restrict__ wo, char* __restrict__ out, char* __restrict__ ws) {
  extern __shared__ __attribute__((aligned(16))) char lds[];
  const unsigned ldsb = (unsigned)(uintptr_t)lds;
  const unsigned lane = threadIdx.x & 63u;
  const unsigned wid = __builtin_amdgcn_readfirstlane(threadIdx.x >> 6);
  const unsigned kvh = blockIdx.x & 7u, half = (blockIdx.x >> 3) & 1u, b = blockIdx.x >> 4;
  const unsigned jw = blockIdx.x & 15u;

  if (wid >= 4) {
    const unsigned hw = wid - 4u;
    const unsigned r4 = lane >> 4, c16 = lane & 15u;
    const unsigned swz = 16u * (c16 ^ (4u * hw + r4));
    const unsigned r8 = (lane >> 2) & 7u, cb = 64u * (lane >> 5) + 16u * (lane & 3u);
    const unsigned ld = ldsb + hw * 1024u;
    const unsigned rq = (b + 2u * half) & 3u, r2 = (b >> 2) & 3u, ro = b & 7u;
    const __amdgpu_buffer_rsrc_t sx = mksrd(x + (size_t)b * SQ * DM * 2, SQ * DM * 2);
    const __amdgpu_buffer_rsrc_t swq = mksrd(wq + (size_t)(kvh * 512 + half * 256) * 2, DM * HQ * DH * 2 - (kvh * 512 + half * 256) * 2);
    const unsigned vo_x = (4u * hw + r4) * 1024u + swz;
    const unsigned vo_wq = r8 * 8192u + 128u * hw + cb;
#define DMA_X(c)  dma4<W_NT, 4096>(sx, vo_x, (((c) + rq) & 3u) * 256u, 16u * 1024u, 32u * 1024u, ld + x_slot_rt(c))
#define DMA_WQ(i) dma4<W_NT, 4096>(swq, vo_wq, (4u * ((((i) >> 2) + rq) & 3u) + (((i) + r2) & 3u)) * 32u * 8192u, 8u * 8192u, 16u * 8192u, ld + wq_slot_rt(i))
    TSH(26);
    DMA_X(0u);
#pragma nounroll
    for (unsigned i = 0; i < 3; ++i) DMA_WQ(i);
    TSH(28);
    const __amdgpu_buffer_rsrc_t sk = mksrd(kc + (size_t)(b * HKV + kvh) * SK * DH * 2, SK * DH * 2);
    const __amdgpu_buffer_rsrc_t sv = mksrd(vc + (size_t)(b * HKV + kvh) * SK * DH * 2, SK * DH * 2);
    const __amdgpu_buffer_rsrc_t swo = mksrd(wo + (size_t)(kvh * 512 + half * 256) * 1024, 256 * 1024);
    const unsigned vo_k = (4u * hw + r4) * 256u + swz;
    const unsigned vo_v = ((hw >> 1) * 8u + r8) * 256u + 128u * (hw & 1u) + cb;
    const unsigned vo_wo = (hw * 8u + r8) * 1024u + cb;
#define DMA_K(t)  dma4<KV_NT, 4096>(sk, vo_k, (t) * 16384u, 4096u, 8192u, ld + ((t) & 3u) * SLOT)
#define DMA_V(t, vs)  dma4<KV_NT, 4096>(sv, vo_v, (t) * 16384u, 4096u, 8192u, ld + (4u + (vs)) * SLOT)
#define DMA_WO(n) dma4<W_NT, 4096>(swo, vo_wo, ((n) & 1u) * 128u * 1024u + ((((n) >> 1) + ro) & 7u) * 128u, 32u * 1024u, 64u * 1024u, ld + wo_slot_rt(n))
    VMW(8);  BAR(); TSH(30); DMA_WQ(3u); DMA_X(1u);
    VMW(12); BAR(); DMA_WQ(4u);
    VMW(12); BAR(); DMA_WQ(5u);
    VMW(12); BAR(); DMA_WQ(6u);
    VMW(8);  BAR(); DMA_WQ(7u);
    VMW(8);  BAR(); DMA_WQ(8u); DMA_X(2u);
    VMW(12); BAR(); DMA_WQ(9u);
    VMW(12); BAR(); DMA_WQ(10u);
    VMW(8);  BAR(); DMA_WQ(11u);
    VMW(8);  BAR(); DMA_WQ(12u); DMA_X(3u);
    VMW(12); BAR(); DMA_WQ(13u);
    VMW(12); BAR(); DMA_WQ(14u); DMA_K(0u);
    VMW(12); BAR(); DMA_WQ(15u);
    VMW(12); BAR(); DMA_K(1u);
    VMW(12); BAR(); DMA_V(0u, 0u);
    VMW(8);  BAR(); DMA_V(1u, 1u);
    VMW(12);
    {
      BAR();
      DMA_K(2u); DMA_K(3u); DMA_V(2u, 2u);
      VMW(16);
      BAR();
      DMA_K(4u); DMA_V(3u, 3u);
      VMW(16);
      unsigned vi = 4;
#pragma nounroll
      for (unsigned u = 2; u <= 12; ++u) {
        BAR();
        DMA_K(u + 3u); DMA_V(u + 2u, vi);
        VMW(16);
        vi = vi == 5u ? 0u : vi + 1u;
      }
      BAR(); DMA_V(15u, vi); DMA_WO(0u); DMA_WO(1u);
      VMW(20);
      BAR(); DMA_WO(2u); DMA_WO(3u);
      VMW(20);
      BAR(); DMA_WO(4u); DMA_WO(5u);
      VMW(24);
    }
    BAR(); DMA_WO(6u); DMA_WO(7u);
    BAR();
    VMW(24); BAR();
    VMW(16); BAR();
    VMW(8);  BAR();
#pragma nounroll
    for (unsigned n = 8; n < 12; ++n) DMA_WO(n);
    VMW(16); BAR();
    DMA_WO(12u); DMA_WO(13u);
    VMW(16); BAR();
    DMA_WO(14u); DMA_WO(15u);
    VMW(16); BAR();
    VMW(8);  BAR();
    VMW(0);  BAR();
    GAS unsigned* ep = (GAS unsigned*)(uintptr_t)(ws + WS_EPOCH + b * 128u);
    unsigned tag = 0;
    if (wid == 4) tag = __hip_atomic_load(ep, __ATOMIC_RELAXED, __HIP_MEMORY_SCOPE_AGENT) + 1u;
    BAR();
    BAR();
    if (wid == 4) {
      GAS unsigned* fl = (GAS unsigned*)(uintptr_t)(ws + WS_FLAG + (b * 16u) * 128u);
      tag = __builtin_amdgcn_readfirstlane(tag);
      if (lane == 0) __hip_atomic_store(fl + jw * 32u, tag, __ATOMIC_RELAXED, __HIP_MEMORY_SCOPE_AGENT);
      for (unsigned spins = 0; spins < (1u << 22); ++spins) {
        const unsigned v = __hip_atomic_load(fl + (lane & 15u) * 32u, __ATOMIC_RELAXED, __HIP_MEMORY_SCOPE_AGENT);
        if (__all(v == tag)) break;
        __builtin_amdgcn_s_sleep(1);
      }
      if (lane == 0 && jw == 0) __hip_atomic_store(ep, tag, __ATOMIC_RELAXED, __HIP_MEMORY_SCOPE_AGENT);
      __builtin_amdgcn_fence(__ATOMIC_ACQUIRE, "wavefront");
    }
    BAR();
    return;
  }

  TS(0);
  const unsigned hh = wid >> 1, qh = wid & 1u, l31 = lane & 31u, h = lane >> 5;
  CW c;
#pragma unroll
  for (int s = 0; s < 8; ++s) c.ka[s] = ldsb + l31 * 256u + 16u * ((2u * s + h) ^ (lane & 15u));
  const unsigned trq = (lane >> 2) & 3u, trp = lane & 3u, trb = (lane >> 4) & 1u;
  const unsigned tr_nat = ldsb + 64u * trq + 32u * trb + 8u * (((trp & 1u) << 1) | (trp >> 1));
  const unsigned tr_v = ldsb + 64u * trq + 32u * trb + 8u * trp + 256u * h;

  {
    QW q;
#pragma unroll
    for (int db = 0; db < 4; ++db) q.acc[db] = f32x16{};
    const unsigned wa = tr_nat + h * 4096u + hh * 2048u;
    const unsigned r2 = (b >> 2) & 3u;
    auto wbase = [&](unsigned i) { unsigned a = wa + wq_slot_rt(i); asm volatile("" : "+v"(a)); return a; };
    q_step<0, false>(q, c, wbase(0u), x_slot_rt(0) + qh * 8192u, r2);
    TS(1);
#pragma nounroll
    for (unsigned cc = 0; cc < 4; ++cc) {
      const unsigned xo = x_slot_rt(cc) + qh * 8192u, xn = x_slot_rt((cc + 1u) & 3u) + qh * 8192u;
      q_step<1, true>(q, c, wbase(4u * cc + 1u), xo, r2);
      q_step<2, true>(q, c, wbase(4u * cc + 2u), xo, r2);
      q_step<3, true>(q, c, wbase(4u * cc + 3u), xo, r2);
      q_step<0, true>(q, c, wbase((4u * cc + 4u) & 15u), xn, r2);
    }
    TS(4);
#pragma unroll
    for (int n = 0; n < LA; ++n) c.kf[n] = ldsb128(c.ka[n]);
#pragma unroll
    for (int db = 0; db < 4; ++db)
#pragma unroll
      for (int u = 0; u < 2; ++u) {
        u32x4 w;
#pragma unroll
        for (int e = 0; e < 4; ++e) w[e] = cvtpk(q.acc[db][8 * u + 2 * e] * QSCALE, q.acc[db][8 * u + 2 * e + 1] * QSCALE);
        c.qf[2 * db + u] = __builtin_bit_cast(bf16x8, w);
      }
  }
  TS(5);
#pragma unroll
  for (int db = 0; db < 4; ++db) c.o[db] = f32x16{};
#pragma unroll
  for (int r = 0; r < 16; ++r) { c.S[1][0][r] = -3.0e38f; c.S[1][1][r] = -3.0e38f; c.negm[r] = 0.f; }
  c.l = 0.f; c.ps = 0.f; c.mx = 0.f; c.seed = -3.0e38f;
  asm volatile("s_nop 1" : "+v"(c.negm), "+v"(c.qf[0]));
  unsigned vslot = 5;
  auto vnext = [&]() { const unsigned a = tr_v + (4u + vslot) * SLOT; vslot = vslot == 5 ? 0 : vslot + 1; return a; };
  SB();
#pragma nounroll
  for (int t = 0; t < 16; t += 2) {
    att_period<0, true, true>(c, vnext(), t == 0 ? -__builtin_inff() : THR);
    att_period<1, true, true>(c, vnext(), THR);
  }
  TS(10);
  att_period<0, false, true>(c, vnext(), THR);
  TS(11);

  {
    auto rr = __builtin_amdgcn_permlane32_swap(__float_as_uint(c.l), __float_as_uint(c.l), false, false);
    c.l = __uint_as_float(rr[0]) + __uint_as_float(rr[1]);
  }
  const float il = 1.0f / c.l;
  OW w;
  BARL();
  {
    const unsigned xw = ldsb + 6u * SLOT + wid * 8192u + lane * 16u;
    const unsigned xr = ldsb + 6u * SLOT + (wid ^ 2u) * 8192u + lane * 16u;
    bf16x8 own[8];
#pragma unroll
    for (int db = 0; db < 4; ++db)
#pragma unroll
      for (int u = 0; u < 2; ++u) {
        u32x4 v;
#pragma unroll
        for (int e = 0; e < 4; ++e) v[e] = cvtpk(c.o[db][8 * u + 2 * e] * il, c.o[db][8 * u + 2 * e + 1] * il);
        own[2 * db + u] = __builtin_bit_cast(bf16x8, v);
        *(LAS bf16x8*)(uintptr_t)(xw + (2 * db + u) * 1024u) = own[2 * db + u];
      }
    BARL();
    TS(12);
#pragma unroll
    for (int s = 0; s < 8; ++s) {
      const bf16x8 oth = ldsb128(xr + s * 1024u);
      w.of[s] = hh == 0 ? own[s] : oth;
      w.of[8 + s] = hh == 0 ? oth : own[s];
    }
  }
  const __amdgpu_buffer_rsrc_t ssl = mksrd(ws + WS_SLAB, 256u * SLAB_WG);
  {
    const unsigned wa = tr_v + hh * 512u;
    const unsigned ro = b & 7u;
    const unsigned voff = lane * 16u, sbase = blockIdx.x * SLAB_WG + wid * FRAG_B;
    w.acc[0] = f32x16{}; w.acc[1] = f32x16{};
    o_step_<0, false>(w, wa + wo_slot_rt(0), wa + wo_slot_rt(1), ssl, voff, 0u, mkseq<16>{});
#pragma nounroll
    for (unsigned C = 1; C < 9; C += 2) {
      BAR(); SB();
      o_step_<1, true>(w, wa + wo_slot_rt(2 * C), wa + wo_slot_rt(2 * C + 1), ssl, voff, sbase + (C == 1 ? 32u : 4u * ((C - 2u + ro) & 7u)) * FRAG_B, mkseq<16>{});
      BAR(); SB();
      o_step_<0, true>(w, wa + wo_slot_rt(C == 7 ? 0u : 2 * C + 2), wa + wo_slot_rt(C == 7 ? 1u : 2 * C + 3), ssl, voff, sbase + 4u * ((C - 1u + ro) & 7u) * FRAG_B, mkseq<16>{});
    }
    TS(14);
    o_store(w.acc[1], ssl, voff, sbase + 4u * ((7u + ro) & 7u) * FRAG_B, 0);
    o_store(w.acc[1], ssl, voff, sbase + 4u * ((7u + ro) & 7u) * FRAG_B, 1);
  }
  TS(24);
  VMW(0);
  TS(25);
  BAR();
  BAR();
  TS(16);
  {
    const unsigned f = wid >> 1, u = wid & 1u;
    const unsigned frag = (jw >> 1) * 4u + 2u * (jw & 1u) + f;
    const unsigned voff = u * 1024u + lane * 16u, soff = b * 16u * SLAB_WG + frag * FRAG_B;
    u32x4 v[16];
#pragma unroll
    for (int p = 0; p < 16; ++p) v[p] = __builtin_amdgcn_raw_buffer_load_b128(ssl, voff, soff + p * SLAB_WG, 16  );
    float s[8] = {0.f, 0.f, 0.f, 0.f, 0.f, 0.f, 0.f, 0.f};
#pragma unroll
    for (int p = 0; p < 16; ++p)
#pragma unroll
      for (int e = 0; e < 4; ++e) { s[2 * e] += __uint_as_float(v[p][e] << 16); s[2 * e + 1] += __uint_as_float(v[p][e] & 0xffff0000u); }
    unsigned a0 = cvtpk(s[0], s[1]), a1 = cvtpk(s[2], s[3]), b0 = cvtpk(s[4], s[5]), b1 = cvtpk(s[6], s[7]);
    auto r0 = __builtin_amdgcn_permlane32_swap(a0, b0, false, false);
    auto r1 = __builtin_amdgcn_permlane32_swap(a1, b1, false, false);
    const u32x4 o4 = {r0[0], r1[0], r0[1], r1[1]};
    const unsigned q = 32u * f + l31, n = 32u * jw + 16u * u + 8u * h;
    *(GAS u32x4*)(uintptr_t)(out + ((size_t)(b * SQ + q) * DM + n) * 2) = o4;
  }
}

extern "C" void kernel_launch(void* const* d_in, const int* in_sizes, int n_in, void* d_out, int out_size, void* d_ws, size_t ws_size, hipStream_t stream) {
  static int ok = 0;
  if (!ok) { (void)hipFuncSetAttribute((const void*)fused_kernel, hipFuncAttributeMaxDynamicSharedMemorySize, (int)LDS_BYTES); ok = 1; }
  hipLaunchKernelGGL(fused_kernel, dim3(256), dim3(512), LDS_BYTES, stream, (const char*)d_in[0], (const char*)d_in[1], (const char*)d_in[2],
                     (const char*)d_in[3], (const char*)d_in[4], (char*)d_out, (char*)d_ws);
}
```
